```python
import torch

def _softmax(x, axis=-1):
    return torch.softmax(x, dim=axis)
def _gelu(x):
    return torch.nn.functional.gelu(x, approximate="tanh")
def _silu(x):
    return torch.nn.functional.silu(x)
def _sigmoid(x):
    return torch.sigmoid(x)
def _layernorm(x, axis=-1, eps=1e-5):
    m = x.mean(dim=axis, keepdim=True)
    v = x.var(dim=axis, keepdim=True, unbiased=False)
    return (x - m) / torch.sqrt(v + eps)
def _scatter_add(tgt, idx, src, axis=0):
    return tgt.clone().index_add_(axis, idx, src)
def _segment_sum(src, seg, n):
    out = torch.zeros((n,) + src.shape[1:], dtype=src.dtype)
    return out.index_add_(0, seg, src)
def _pad(x, pw):
    flat = tuple(v for pair in reversed(pw) for v in pair)
    return torch.nn.functional.pad(x, flat)

kernel_name = 'standalone__mha_block_cached__near_ridge'

def setup_inputs():

    _w = (lambda a: (lambda t: t.to(torch.bfloat16) if t.is_floating_point() else t)(torch.as_tensor(a)))
    B, Sq, D = 8, 192, 448
    H, Sk, Dh = 16, 1024, 256
    return {
        "x": _w((lambda *s: torch.randn(*s, dtype=torch.bfloat16) * 0.8)(B, Sq, D)),
        "k_cache": _w((lambda *s: torch.randn(*s, dtype=torch.bfloat16) * 0.8)(B, H, Sk, Dh)),
        "v_cache": _w((lambda *s: torch.randn(*s, dtype=torch.bfloat16) * 0.05)(B, H, Sk, Dh)),
        "wq": _w((lambda *s: torch.randn(*s, dtype=torch.bfloat16) * 0.8)(D, H * Dh) * (D ** -0.5)),
        "wo": _w((lambda *s: torch.randn(*s, dtype=torch.bfloat16) * 0.05)(H * Dh, D) * ((H * Dh) ** -0.5)),
    }

def reference(x, k_cache, v_cache, wq, wo):
    B, Sq, D = x.shape
    H, Dh = 16, 256
    q = (lambda x, *d: x.permute(*d))((x @ wq).reshape(B, Sq, H, Dh), 0, 2, 1, 3)
    scores = torch.einsum("bhqd,bhkd->bhqk", q, k_cache) * (Dh ** -0.5)
    p = _softmax(scores, axis=-1)
    o = torch.einsum("bhqk,bhkd->bhqd", p, v_cache)
    return (lambda x, *d: x.permute(*d))(o, 0, 2, 1, 3).reshape(B, Sq, H * Dh) @ wo
```

```cpp
#include <hip/hip_runtime.h>
#include <hip/hip_bf16.h>

typedef unsigned short u16;
typedef __attribute__((ext_vector_type(8))) short bf16x8;
typedef __attribute__((ext_vector_type(4))) short bf16x4;
typedef __attribute__((ext_vector_type(4))) float f32x4;
typedef __attribute__((ext_vector_type(2))) float f32x2;
typedef __attribute__((ext_vector_type(2))) __bf16 bf16x2_t;
typedef __attribute__((ext_vector_type(4))) unsigned u32x4;
typedef __attribute__((ext_vector_type(2))) unsigned u32x2;
typedef __attribute__((address_space(3))) bf16x4* lds_b64_ptr;

#define DI __device__ __forceinline__

constexpr int NB = 8, SQ = 192, DM = 448, NH = 16, SK = 1024, DH = 256;
constexpr int HD = NH * DH;

DI unsigned cvtpk(float lo, float hi) {
  f32x2 v = {lo, hi};
  bf16x2_t b = __builtin_convertvector(v, bf16x2_t);
  return __builtin_bit_cast(unsigned, b);
}
DI bf16x4 tr_read(const char* p) {
  return __builtin_amdgcn_ds_read_tr16_b64_v4i16((lds_b64_ptr)(p));
}

__global__ __launch_bounds__(256) void gemm_simple(const u16* __restrict__ A, const u16* __restrict__ Bm,
                                                   u16* __restrict__ C, int K, int lda, int ldb, int ldc) {
  __shared__ __attribute__((aligned(16))) char sA[64 * 144];
  __shared__ __attribute__((aligned(16))) char sB[64 * 144];
  const int tid = threadIdx.x, lane = tid & 63, wid = tid >> 6;
  const int g = lane >> 4, i16 = lane & 15;
  const int m0 = blockIdx.y * 64, n0 = blockIdx.x * 64;
  f32x4 acc[4] = {};
  for (int k0 = 0; k0 < K; k0 += 64) {
    __syncthreads();
#pragma unroll
    for (int it = 0; it < 2; ++it) {
      int idx = tid + it * 256, r = idx >> 3, c = idx & 7;
      *(u32x4*)(sA + r * 144 + c * 16) = *(const u32x4*)(A + (size_t)(m0 + r) * lda + k0 + c * 8);
      *(u32x4*)(sB + r * 144 + c * 16) = *(const u32x4*)(Bm + (size_t)(k0 + r) * ldb + n0 + c * 8);
    }
    __syncthreads();
#pragma unroll
    for (int kk = 0; kk < 2; ++kk) {
      bf16x8 af = *(const bf16x8*)(sA + (wid * 16 + i16) * 144 + kk * 64 + g * 16);
#pragma unroll
      for (int nb = 0; nb < 4; ++nb) {
        const char* p = sB + (kk * 32 + g * 8 + (i16 >> 2)) * 144 + nb * 32 + (i16 & 3) * 8;
        bf16x4 lo = tr_read(p), hi = tr_read(p + 4 * 144);
        bf16x8 bf = __builtin_shufflevector(lo, hi, 0, 1, 2, 3, 4, 5, 6, 7);
        acc[nb] = __builtin_amdgcn_mfma_f32_16x16x32_bf16(bf, af, acc[nb], 0, 0, 0);
      }
    }
  }
#pragma unroll
  for (int nb = 0; nb < 4; ++nb) {
    u32x2 w = {cvtpk(acc[nb][0], acc[nb][1]), cvtpk(acc[nb][2], acc[nb][3])};
    *(u32x2*)(C + (size_t)(m0 + wid * 16 + i16) * ldc + n0 + nb * 16 + g * 4) = w;
  }
}

constexpr int SLOT = 32768;
constexpr int NSLOT = 5;
constexpr int ATT_LDS = SLOT * NSLOT;
constexpr float SCL = 0.0625f * 1.4426950408889634f;
constexpr float THR = 40.0f;

#define WAIT_VM(n) asm volatile("s_waitcnt vmcnt(" #n ") lgkmcnt(0)" ::: "memory")
#define BARRIER() __builtin_amdgcn_s_barrier()

#define SCHED() __builtin_amdgcn_sched_barrier(0)
DI void mfma_acc(f32x4& d, bf16x8 a, bf16x8 b) {
  asm volatile("v_mfma_f32_16x16x32_bf16 %0, %1, %2, %0" : "+a"(d) : "v"(a), "v"(b));
}
DI void mfma_v(f32x4& d, bf16x8 a, bf16x8 b) {
  asm volatile("v_mfma_f32_16x16x32_bf16 %0, %1, %2, %0" : "+v"(d) : "v"(a), "v"(b));
}
DI void mfma_v0(f32x4& d, bf16x8 a, bf16x8 b) {
  asm volatile("v_mfma_f32_16x16x32_bf16 %0, %1, %2, 0" : "=&v"(d) : "v"(a), "v"(b));
}

DI void dma_item(unsigned ldsdst, unsigned voff0, int sh, __amdgpu_buffer_rsrc_t rsrc, unsigned soff, unsigned lane) {
  const unsigned hi = lane >> 5, l32 = lane & 31;
#pragma unroll
  for (int j = 0; j < 8; ++j) {
    unsigned r = 2 * j + hi;
    unsigned sw = sh ? ((r & 7) << 1) : r;
    unsigned voff = voff0 + 16 * (l32 ^ sw);
    asm volatile("s_mov_b32 m0, %0\n\ts_nop 0\n\tbuffer_load_dwordx4 %1, %2, %3 offen lds"
                 :: "s"(ldsdst + 1024u * j), "v"(voff), "s"(rsrc), "s"(soff + 1024u * j) : "memory");
  }
}

__global__ __launch_bounds__(256, 1) void attn_kernel(const u16* __restrict__ Qg, const u16* __restrict__ Kc,
                                                      const u16* __restrict__ Vc, u16* __restrict__ Og) {
  extern __shared__ __attribute__((aligned(16))) char smem[];
  const int tid = threadIdx.x, lane = tid & 63;
  const int wid = __builtin_amdgcn_readfirstlane(tid >> 6);
  const int rg = wid & 1, kh = wid >> 1;
  const int bid = blockIdx.x;
  const int b = bid & 7, idx = bid >> 3, h = idx >> 1, rh = idx & 1;
  const int g = lane >> 4, i16 = lane & 15;
  const unsigned ldsb = (unsigned)(size_t)smem;

  const char* Kbase = (const char*)Kc + (size_t)(b * NH + h) * SK * DH * 2;
  const char* Vbase = (const char*)Vc + (size_t)(b * NH + h) * SK * DH * 2;
  __amdgpu_buffer_rsrc_t rK = __builtin_amdgcn_make_buffer_rsrc((void*)Kbase, (short)0, SK * DH * 2, 0x00020000);
  __amdgpu_buffer_rsrc_t rV = __builtin_amdgcn_make_buffer_rsrc((void*)Vbase, (short)0, SK * DH * 2, 0x00020000);
  const unsigned voff0 = (unsigned)(lane >> 5) * 512u;
  const unsigned wsoff = (unsigned)wid * 8192u;

  auto issue = [&](int item, int slot) {
    unsigned dst = ldsb + (unsigned)slot * SLOT + wsoff;
    unsigned so = (unsigned)(item >> 1) * 32768u + wsoff;
    if (item & 1) dma_item(dst, voff0, 1, rV, so, lane);
    else          dma_item(dst, voff0, 0, rK, so, lane);
  };

  const int q0 = rh * 96 + rg * 48;
  bf16x8 qf[3][8];
  {
    const u16* qp = Qg + (size_t)(b * SQ + q0 + i16) * HD + h * DH + g * 8;
#pragma unroll
    for (int qb = 0; qb < 3; ++qb)
#pragma unroll
      for (int ks = 0; ks < 8; ++ks) qf[qb][ks] = *(const bf16x8*)(qp + (size_t)qb * 16 * HD + ks * 32);
  }

  f32x4 o[3][16];
  float mrun[3] = {0.f, 0.f, 0.f}, lsum[3], mtrue[3];
  int attempt = 0;

  unsigned koff[4], voff[8];
#pragma unroll
  for (int a = 0; a < 4; ++a) koff[a] = (unsigned)(kh * 32 + i16) * 512u + 16u * (unsigned)((4 * a + g) ^ i16);
  {
    const int row = 4 * g + (i16 >> 2), r7 = row & 7;
#pragma unroll
    for (int bb = 0; bb < 8; ++bb) voff[bb] = (unsigned)(kh * 32 + row) * 512u + 32u * (unsigned)(bb ^ r7) + 8u * (unsigned)(i16 & 3);
  }

  int sl_c = 0;
  int sl_i = 4;

  auto tile = [&](int t, auto wk_tag, auto wv_tag, auto isk_tag, auto isv_tag) {
    constexpr int WK = decltype(wk_tag)::value, WV = decltype(wv_tag)::value;
    constexpr bool ISK = decltype(isk_tag)::value, ISV = decltype(isv_tag)::value;
    if constexpr (WK == 24) WAIT_VM(24); else if constexpr (WK == 8) WAIT_VM(8); else WAIT_VM(0);
    BARRIER();
    f32x4 s[3][2];
    {
      const char* kb0 = smem + (unsigned)sl_c * SLOT;
      constexpr int PF = 4;
      bf16x8 kf[16];
      auto kread = [&](int i) { kf[i] = *(const bf16x8*)(kb0 + koff[(i >> 1) & 3] + (i >> 3) * 256 + (i & 1) * 8192); };
#pragma unroll
      for (int i = 0; i < PF; ++i) kread(i);
      SCHED();
      if constexpr (ISK) { issue(2 * t + 4, sl_i); sl_i = (sl_i == NSLOT - 1) ? 0 : sl_i + 1; }
      SCHED();
#pragma unroll
      for (int i = 0; i < 16; ++i) {
        if (i + PF < 16) kread(i + PF);
        const int ks = i >> 1, kb = i & 1;
#pragma unroll
        for (int qb = 0; qb < 3; ++qb) {
          if (ks == 0) mfma_v0(s[qb][kb], kf[i], qf[qb][ks]);
          else mfma_v(s[qb][kb], kf[i], qf[qb][ks]);
        }
        SCHED();
      }
      asm volatile("s_nop 7" ::: "memory");
#pragma unroll
      for (int qb = 0; qb < 3; ++qb) asm volatile("" : "+v"(s[qb][0]), "+v"(s[qb][1]));
    }
    sl_c = (sl_c == NSLOT - 1) ? 0 : sl_c + 1;
#pragma unroll
    for (int qb = 0; qb < 3; ++qb) {
      float a = fmaxf(fmaxf(s[qb][0][0], s[qb][0][1]), fmaxf(s[qb][0][2], s[qb][0][3]));
      float c = fmaxf(fmaxf(s[qb][1][0], s[qb][1][1]), fmaxf(s[qb][1][2], s[qb][1][3]));
      mtrue[qb] = fmaxf(mtrue[qb], fmaxf(a, c));
    }
    if (t == 0 && attempt == 0) {
#pragma unroll
      for (int qb = 0; qb < 3; ++qb) {
        float r = mtrue[qb];
        r = fmaxf(r, __shfl_xor(r, 16));
        r = fmaxf(r, __shfl_xor(r, 32));
        mrun[qb] = r * SCL;
      }
    }
    bf16x8 pf[3];
#pragma unroll
    for (int qb = 0; qb < 3; ++qb) {
      float p[8];
#pragma unroll
      for (int e = 0; e < 8; ++e) p[e] = __builtin_amdgcn_exp2f(fmaf(s[qb][e >> 2][e & 3], SCL, -mrun[qb]));
      lsum[qb] += ((p[0] + p[1]) + (p[2] + p[3])) + ((p[4] + p[5]) + (p[6] + p[7]));
      u32x4 w = {cvtpk(p[0], p[1]), cvtpk(p[2], p[3]), cvtpk(p[4], p[5]), cvtpk(p[6], p[7])};
      pf[qb] = __builtin_bit_cast(bf16x8, w);
    }
    if constexpr (WV == 24) WAIT_VM(24); else if constexpr (WV == 16) WAIT_VM(16); else WAIT_VM(0);
    BARRIER();
    {
      const char* vb0 = smem + (unsigned)sl_c * SLOT;
      constexpr int PF = 4;
      bf16x4 vlo[16], vhi[16];
      auto vread = [&](int db) {
        const char* p = vb0 + voff[db & 7] + (db >> 3) * 256;
        vlo[db] = tr_read(p); vhi[db] = tr_read(p + 8192);
      };
#pragma unroll
      for (int i = 0; i < PF; ++i) vread(i);
      SCHED();
      if constexpr (ISV) { issue(2 * t + 5, sl_i); sl_i = (sl_i == NSLOT - 1) ? 0 : sl_i + 1; }
      asm volatile("s_nop 1" ::: "memory");
      SCHED();
#pragma unroll
      for (int db = 0; db < 16; ++db) {
        if (db + PF < 16) vread(db + PF);
        bf16x8 vf = __builtin_shufflevector(vlo[db], vhi[db], 0, 1, 2, 3, 4, 5, 6, 7);
#pragma unroll
        for (int qb = 0; qb < 3; ++qb) mfma_acc(o[qb][db], vf, pf[qb]);
        SCHED();
      }
    }
    sl_c = (sl_c == NSLOT - 1) ? 0 : sl_c + 1;
  };
  using I24 = std::integral_constant<int, 24>;
  using I16 = std::integral_constant<int, 16>;
  using I8 = std::integral_constant<int, 8>;
  using I0 = std::integral_constant<int, 0>;
  using T_ = std::true_type;
  using F_ = std::false_type;
  for (;; ++attempt) {
    issue(0, 0); issue(1, 1); issue(2, 2); issue(3, 3);
    sl_c = 0; sl_i = 4;
#pragma unroll
    for (int qb = 0; qb < 3; ++qb)
#pragma unroll
      for (int ks = 0; ks < 8; ++ks) asm volatile("" : "+v"(qf[qb][ks]));
#pragma unroll
    for (int qb = 0; qb < 3; ++qb) {
      lsum[qb] = 0.f; mtrue[qb] = -3.0e38f;
#pragma unroll
      for (int db = 0; db < 16; ++db) o[qb][db] = (f32x4){0.f, 0.f, 0.f, 0.f};
    }
#pragma unroll 1
    for (int t = 0; t < 14; ++t) tile(t, I24{}, I24{}, T_{}, T_{});
    tile(14, I24{}, I16{}, F_{}, F_{});
    tile(15, I8{}, I0{}, F_{}, F_{});
    if (attempt) break;
    bool bad = false;
#pragma unroll
    for (int qb = 0; qb < 3; ++qb) {
      float r = mtrue[qb];
      r = fmaxf(r, __shfl_xor(r, 16));
      r = fmaxf(r, __shfl_xor(r, 32));
      mtrue[qb] = r * SCL;
      bad |= !(mtrue[qb] <= mrun[qb] + THR);
    }
    {
      int* flags = (int*)(smem + 4 * SLOT);
      int wbad = __any(bad) ? 1 : 0;
      asm volatile("s_waitcnt lgkmcnt(0)" ::: "memory");
      BARRIER();
      if (lane == 0) flags[wid] = wbad;
      asm volatile("s_waitcnt lgkmcnt(0)" ::: "memory");
      BARRIER();
      int f = flags[0] | flags[1] | flags[2] | flags[3];
      f = __builtin_amdgcn_readfirstlane(f);
      if (!f) break;
    }
#pragma unroll
    for (int qb = 0; qb < 3; ++qb) mrun[qb] = mtrue[qb];
  }

  asm volatile("s_nop 7\n\ts_nop 7" ::: "memory");
#pragma unroll
  for (int qb = 0; qb < 3; ++qb) {
    float r = lsum[qb];
    r += __shfl_xor(r, 16);
    r += __shfl_xor(r, 32);
    lsum[qb] = r;
  }
  asm volatile("s_waitcnt lgkmcnt(0)" ::: "memory");
  BARRIER();
  char* myreg = smem + wid * 32768;
  char* preg = smem + (wid ^ 2) * 32768;
#pragma unroll
  for (int qb = 0; qb < 3; ++qb) {
#pragma unroll
    for (int d = 0; d < 8; ++d) {
      f32x4 v = kh ? o[qb][d] : o[qb][8 + d];
      *(f32x4*)(myreg + (qb * 8 + d) * 1024 + lane * 16) = v;
    }
    *(f32x2*)(myreg + 24576 + qb * 512 + lane * 8) = (f32x2){mrun[qb], lsum[qb]};
  }
  __syncthreads();
  u16* op = Og + (size_t)(b * SQ + q0 + i16) * HD + h * DH + kh * 128 + g * 4;
#pragma unroll
  for (int qb = 0; qb < 3; ++qb) {
    f32x2 ml = *(const f32x2*)(preg + 24576 + qb * 512 + lane * 8);
    float mn = fmaxf(mrun[qb], ml[0]);
    float a0 = __builtin_amdgcn_exp2f(mrun[qb] - mn), a1 = __builtin_amdgcn_exp2f(ml[0] - mn);
    float inv = 1.0f / (lsum[qb] * a0 + ml[1] * a1);
    a0 *= inv; a1 *= inv;
#pragma unroll
    for (int d = 0; d < 8; ++d) {
      f32x4 pv = *(const f32x4*)(preg + (qb * 8 + d) * 1024 + lane * 16);
      f32x4 mine = kh ? o[qb][8 + d] : o[qb][d];
      f32x4 r = mine * a0 + pv * a1;
      u32x2 w = {cvtpk(r[0], r[1]), cvtpk(r[2], r[3])};
      *(u32x2*)(op + (size_t)qb * 16 * HD + d * 16) = w;
    }
  }
}

extern "C" void kernel_launch(void* const* d_in, const int* in_sizes, int n_in, void* d_out, int out_size,
                              void* d_ws, size_t ws_size, hipStream_t stream) {
  const u16* x = (const u16*)d_in[0];
  const u16* kc = (const u16*)d_in[1];
  const u16* vc = (const u16*)d_in[2];
  const u16* wq = (const u16*)d_in[3];
  const u16* wo = (const u16*)d_in[4];
  u16* out = (u16*)d_out;
  u16* Qb = (u16*)d_ws;
  u16* Ob = Qb + (size_t)NB * SQ * HD;
  hipFuncSetAttribute((const void*)attn_kernel, hipFuncAttributeMaxDynamicSharedMemorySize, ATT_LDS);
  gemm_simple<<<dim3(HD / 64, NB * SQ / 64), 256, 0, stream>>>(x, wq, Qb, DM, DM, HD, HD);
  attn_kernel<<<256, 256, ATT_LDS, stream>>>(Qb, kc, vc, Ob);
  gemm_simple<<<dim3(DM / 64, NB * SQ / 64), 256, 0, stream>>>(Ob, wo, out, HD, HD, DM, DM);
}
```

```cpp
#include <hip/hip_runtime.h>
#include <hip/hip_bf16.h>

typedef unsigned short u16;
typedef __attribute__((ext_vector_type(8))) short bf16x8;
typedef __attribute__((ext_vector_type(4))) short bf16x4;
typedef __attribute__((ext_vector_type(4))) float f32x4;
typedef __attribute__((ext_vector_type(2))) float f32x2;
typedef __attribute__((ext_vector_type(2))) __bf16 bf16x2_t;
typedef __attribute__((ext_vector_type(4))) unsigned u32x4;
typedef __attribute__((ext_vector_type(2))) unsigned u32x2;
typedef __attribute__((address_space(3))) bf16x4* lds_b64_ptr;

#define DI __device__ __forceinline__

constexpr int NB = 8, SQ = 192, DM = 448, NH = 16, SK = 1024, DH = 256;
constexpr int HD = NH * DH;

DI unsigned cvtpk(float lo, float hi) {
  f32x2 v = {lo, hi};
  bf16x2_t b = __builtin_convertvector(v, bf16x2_t);
  return __builtin_bit_cast(unsigned, b);
}
DI bf16x4 tr_read(const char* p) {
  return __builtin_amdgcn_ds_read_tr16_b64_v4i16((lds_b64_ptr)(p));
}

__global__ __launch_bounds__(256) void gemm_simple(const u16* __restrict__ A, const u16* __restrict__ Bm,
                                                   u16* __restrict__ C, int K, int lda, int ldb, int ldc) {
  __shared__ __attribute__((aligned(16))) char sA[64 * 144];
  __shared__ __attribute__((aligned(16))) char sB[64 * 144];
  const int tid = threadIdx.x, lane = tid & 63, wid = tid >> 6;
  const int g = lane >> 4, i16 = lane & 15;
  const int m0 = blockIdx.y * 64, n0 = blockIdx.x * 64;
  f32x4 acc[4] = {};
  for (int k0 = 0; k0 < K; k0 += 64) {
    __syncthreads();
#pragma unroll
    for (int it = 0; it < 2; ++it) {
      int idx = tid + it * 256, r = idx >> 3, c = idx & 7;
      *(u32x4*)(sA + r * 144 + c * 16) = *(const u32x4*)(A + (size_t)(m0 + r) * lda + k0 + c * 8);
      *(u32x4*)(sB + r * 144 + c * 16) = *(const u32x4*)(Bm + (size_t)(k0 + r) * ldb + n0 + c * 8);
    }
    __syncthreads();
#pragma unroll
    for (int kk = 0; kk < 2; ++kk) {
      bf16x8 af = *(const bf16x8*)(sA + (wid * 16 + i16) * 144 + kk * 64 + g * 16);
#pragma unroll
      for (int nb = 0; nb < 4; ++nb) {
        const char* p = sB + (kk * 32 + g * 8 + (i16 >> 2)) * 144 + nb * 32 + (i16 & 3) * 8;
        bf16x4 lo = tr_read(p), hi = tr_read(p + 4 * 144);
        bf16x8 bf = __builtin_shufflevector(lo, hi, 0, 1, 2, 3, 4, 5, 6, 7);
        acc[nb] = __builtin_amdgcn_mfma_f32_16x16x32_bf16(bf, af, acc[nb], 0, 0, 0);
      }
    }
  }
#pragma unroll
  for (int nb = 0; nb < 4; ++nb) {
    u32x2 w = {cvtpk(acc[nb][0], acc[nb][1]), cvtpk(acc[nb][2], acc[nb][3])};
    *(u32x2*)(C + (size_t)(m0 + wid * 16 + i16) * ldc + n0 + nb * 16 + g * 4) = w;
  }
}

constexpr int SLOT = 32768;
constexpr int NSLOT = 5;
constexpr int ATT_LDS = SLOT * NSLOT;
constexpr float SCL = 0.0625f * 1.4426950408889634f;
constexpr float THR = 40.0f;

#define WAIT_VM(n) asm volatile("s_waitcnt vmcnt(" #n ") lgkmcnt(0)" ::: "memory")
#define BARRIER() __builtin_amdgcn_s_barrier()

#define SCHED() __builtin_amdgcn_sched_barrier(0)
#define STAMP_END2()
#define STAMP(k)
#define STAMP_INIT()
#define STAMP_END()
DI void mfma_acc(f32x4& d, bf16x8 a, bf16x8 b) {
  asm volatile("v_mfma_f32_16x16x32_bf16 %0, %1, %2, %0" : "+a"(d) : "v"(a), "v"(b));
}
DI void mfma_v(f32x4& d, bf16x8 a, bf16x8 b) {
  asm volatile("v_mfma_f32_16x16x32_bf16 %0, %1, %2, %0" : "+v"(d) : "v"(a), "v"(b));
}
DI void mfma_v0(f32x4& d, bf16x8 a, bf16x8 b) {
  asm volatile("v_mfma_f32_16x16x32_bf16 %0, %1, %2, 0" : "=&v"(d) : "v"(a), "v"(b));
}

DI void mfma_vc(f32x4& d, bf16x8 a, bf16x8 b, f32x4 c) {
  asm volatile("v_mfma_f32_16x16x32_bf16 %0, %1, %2, %3" : "=&v"(d) : "v"(a), "v"(b), "v"(c));
}
#define PIN8A(x) "+a"((x)[0]), "+a"((x)[1]), "+a"((x)[2]), "+a"((x)[3]), "+a"((x)[4]), "+a"((x)[5]), "+a"((x)[6]), "+a"((x)[7])
#define PIN7A(x) "+a"((x)[0]), "+a"((x)[1]), "+a"((x)[2]), "+a"((x)[3]), "+a"((x)[4]), "+a"((x)[5]), "+a"((x)[6])
DI float max3f(float a, float b, float c) { float r; asm("v_max3_f32 %0, %1, %2, %3" : "=v"(r) : "v"(a), "v"(b), "v"(c)); return r; }
DI float fadd_s(float a, float b) { float r; asm("v_add_f32_e32 %0, %1, %2" : "=v"(r) : "v"(a), "v"(b)); return r; }

template <int OFF>
DI void dma_piece(unsigned ldsdst, unsigned voff, __amdgpu_buffer_rsrc_t rsrc, unsigned soff) {
  asm volatile("s_mov_b32 m0, %0\n\ts_nop 0\n\tbuffer_load_dwordx4 %1, %2, %3 offen offset:%4 nt lds"
               :: "s"(ldsdst), "v"(voff), "s"(rsrc), "s"(soff), "i"(OFF) : "memory");
}
DI void dma_piece_w(unsigned ldsdst, unsigned voff, __amdgpu_buffer_rsrc_t rsrc, unsigned soff) {
  asm volatile("s_mov_b32 m0, %0\n\ts_nop 0\n\tbuffer_load_dwordx4 %1, %2, %3 offen lds"
               :: "s"(ldsdst), "v"(voff), "s"(rsrc), "s"(soff) : "memory");
}
DI float xmax_rows(float v) {
  auto a = __builtin_amdgcn_permlane16_swap(__float_as_uint(v), __float_as_uint(v), false, false);
  v = fmaxf(__uint_as_float(a[0]), __uint_as_float(a[1]));
  auto c = __builtin_amdgcn_permlane32_swap(__float_as_uint(v), __float_as_uint(v), false, false);
  return fmaxf(__uint_as_float(c[0]), __uint_as_float(c[1]));
}
DI float xsum_rows(float v) {
  auto a = __builtin_amdgcn_permlane16_swap(__float_as_uint(v), __float_as_uint(v), false, false);
  v = __uint_as_float(a[0]) + __uint_as_float(a[1]);
  auto c = __builtin_amdgcn_permlane32_swap(__float_as_uint(v), __float_as_uint(v), false, false);
  return __uint_as_float(c[0]) + __uint_as_float(c[1]);
}

__global__ __launch_bounds__(256, 1) void attn_kernel(const u16* __restrict__ X, const u16* __restrict__ Wq,
                                                      const u16* __restrict__ Kc, const u16* __restrict__ Vc,
                                                      const u16* __restrict__ Wo, u16* __restrict__ Part) {
  extern __shared__ __attribute__((aligned(16))) char smem[];
  STAMP_INIT();
  const int tid = threadIdx.x, lane = tid & 63;
  const int wid = __builtin_amdgcn_readfirstlane(tid >> 6);
  const int rg = wid & 1, kh = wid >> 1;
  const int bid = blockIdx.x;
  const int idx = bid >> 3, rh = idx & 1, h = 2 * (bid & 7) + ((idx >> 1) & 1), b = idx >> 2;
  const int g = lane >> 4, i16 = lane & 15;
  const unsigned ldsb = (unsigned)(size_t)smem;
  const int q0 = rh * 96 + rg * 48;

  const char* Kbase = (const char*)Kc + (size_t)(b * NH + h) * SK * DH * 2;
  const char* Vbase = (const char*)Vc + (size_t)(b * NH + h) * SK * DH * 2;
  __amdgpu_buffer_rsrc_t rK = __builtin_amdgcn_make_buffer_rsrc((void*)Kbase, (short)0, SK * DH * 2, 0x00020000);
  __amdgpu_buffer_rsrc_t rV = __builtin_amdgcn_make_buffer_rsrc((void*)Vbase, (short)0, SK * DH * 2, 0x00020000);
  __amdgpu_buffer_rsrc_t rW = __builtin_amdgcn_make_buffer_rsrc((void*)Wq, (short)0, DM * HD * 2, 0x00020000);
  const unsigned wsoff = (unsigned)wid * 8192u;
  unsigned kdo[8], vdo[4], wdo[4];
  {
    const unsigned hi = lane >> 5, l32 = lane & 31;
#pragma unroll
    for (int j = 0; j < 8; ++j) kdo[j] = hi * 512u + 16u * (l32 ^ (2u * j + hi));
#pragma unroll
    for (int j = 0; j < 4; ++j) vdo[j] = hi * 512u + 16u * (l32 ^ ((2u * j + hi) << 1));
#pragma unroll
    for (int j = 0; j < 4; ++j) wdo[j] = hi * 8192u + 16u * (l32 ^ (((2u * (j & 1) + hi) | ((j >> 1) << 2)) << 1));
  }
  auto issueK = [&](int tl, int slot, auto jt) {
    constexpr int j = decltype(jt)::value;
    dma_piece<(j & 3) * 1024>(ldsb + (unsigned)slot * SLOT + wsoff + 4096u * (j >> 2), kdo[j], rK,
                              (unsigned)tl * 32768u + wsoff + 4096u * (j >> 2));
  };
  auto issueV = [&](int tl, int slot, auto jt) {
    constexpr int j = decltype(jt)::value;
    dma_piece<(j & 3) * 1024>(ldsb + (unsigned)slot * SLOT + wsoff + 4096u * (j >> 2), vdo[j & 3], rV,
                              (unsigned)tl * 32768u + wsoff + 4096u * (j >> 2));
  };
  auto issueW = [&](int c, int slot, int j) {
    dma_piece_w(ldsb + (unsigned)slot * SLOT + wsoff + 1024u * j, wdo[(j & 1) + 2 * (j >> 2)], rW,
                (unsigned)(64 * c + 16 * wid + 2 * j) * 8192u + (unsigned)h * 512u);
  };
#define FOR8(J, ...) { { constexpr int J = 0; __VA_ARGS__ } { constexpr int J = 1; __VA_ARGS__ } { constexpr int J = 2; __VA_ARGS__ } { constexpr int J = 3; __VA_ARGS__ } \
                       { constexpr int J = 4; __VA_ARGS__ } { constexpr int J = 5; __VA_ARGS__ } { constexpr int J = 6; __VA_ARGS__ } { constexpr int J = 7; __VA_ARGS__ } }
#define IC(x) std::integral_constant<int, (x)>{}

  {
    __amdgpu_buffer_rsrc_t rX = __builtin_amdgcn_make_buffer_rsrc((void*)(X + (size_t)(b * SQ + rh * 96) * DM), (short)0, 96 * DM * 2, 0x00020000);
#pragma unroll
    for (int j = 0; j < 21; ++j) {
      const unsigned n = (unsigned)(wid * 21 + j) * 64u + lane, r = n / 56u, c = n - r * 56u;
      dma_piece_w(ldsb + (unsigned)(wid * 21 + j) * 1024u, r * 896u + 16u * (c ^ ((r >> 1) & 7u)), rX, 0u);
    }
  }
#pragma unroll
  for (int c = 0; c < 2; ++c)
#pragma unroll
    for (int j = 0; j < 8; ++j) issueW(c, c + 3, j);

  unsigned koff[4], voff[8];
#pragma unroll
  for (int a = 0; a < 4; ++a) koff[a] = (unsigned)(kh * 32 + i16) * 512u + 16u * (unsigned)((4 * a + g) ^ i16);
  {
    const int row = 4 * g + (i16 >> 2), r7 = row & 7;
#pragma unroll
    for (int bb = 0; bb < 8; ++bb) voff[bb] = (unsigned)(kh * 32 + row) * 512u + 32u * (unsigned)(bb ^ r7) + 8u * (unsigned)(i16 & 3);
  }

  bf16x8 qf[3][8];
  {
    f32x4 qa[3][8];
#pragma unroll
    for (int qb = 0; qb < 3; ++qb)
#pragma unroll
      for (int db = 0; db < 8; ++db) qa[qb][db] = (f32x4){0.f, 0.f, 0.f, 0.f};
    unsigned woff[8];
    {
      const int q4 = i16 >> 2, f = q4 | ((g & 1) << 2);
#pragma unroll
      for (int db = 0; db < 8; ++db) woff[db] = (unsigned)(8 * g + q4) * 512u + (unsigned)kh * 256u + 32u * (unsigned)(db ^ f) + 8u * (unsigned)(i16 & 3);
    }
    bf16x8 xf[3][14];
    STAMP(0);
    WAIT_VM(16);
    BARRIER();
    STAMP(1);
#pragma unroll
    for (int qb = 0; qb < 3; ++qb) {
      const unsigned r = (unsigned)(rg * 48 + qb * 16 + i16);
#pragma unroll
      for (int kk = 0; kk < 14; ++kk)
        xf[qb][kk] = *(const bf16x8*)(smem + r * 896u + 128u * (kk >> 1) + 16u * ((unsigned)(4 * (kk & 1) + g) ^ ((r >> 1) & 7u)));
    }
#pragma unroll
    for (int c = 0; c < 7; ++c) {
      STAMP(2);
      if (c == 0) WAIT_VM(8); else WAIT_VM(24);
      BARRIER();
      STAMP(3);
      const char* wb0 = smem + ((c + 3) % 5) * SLOT;
      constexpr int PF = 4;
      bf16x4 wlo[16], whi[16];
      auto wread = [&](int i) {
        const char* p = wb0 + woff[i & 7] + (i >> 3) * 16384;
        wlo[i] = tr_read(p); whi[i] = tr_read(p + 2048);
      };
#pragma unroll
      for (int i = 0; i < PF; ++i) wread(i);
      SCHED();
#pragma unroll
      for (int i = 0; i < 16; ++i) {
        bf16x8 wf = __builtin_shufflevector(wlo[i], whi[i], 0, 1, 2, 3, 4, 5, 6, 7);
        if (i + PF < 16) wread(i + PF);
        if (c == 0) {
          if (i < 8) { issueW(2, 0, i); issueW(3, 1, i); } else issueW(4, 2, i - 8);
        } else if (i & 1) {
          if (c < 3) issueW(c + 4, (c + 7) % 5, i >> 1);
          FOR8(J, if (J == (i >> 1)) { if (c == 3) issueK(0, 0, IC(J)); if (c == 4) issueK(1, 1, IC(J)); if (c == 5) issueV(0, 2, IC(J)); })
        }
#pragma unroll
        for (int qb = 0; qb < 3; ++qb) mfma_acc(qa[qb][i & 7], wf, xf[qb][2 * c + (i >> 3)]);
        SCHED();
      }
    }
    STAMP(2);
    asm volatile("s_nop 7\n\ts_nop 7" : PIN8A(qa[0]), PIN8A(qa[1]), PIN8A(qa[2]));
    asm volatile("s_waitcnt lgkmcnt(0)" ::: "memory");
    BARRIER();
#pragma unroll
    for (int qb = 0; qb < 3; ++qb)
#pragma unroll
      for (int db = 0; db < 8; ++db) {
        u32x2 w = {cvtpk(qa[qb][db][0] * SCL, qa[qb][db][1] * SCL), cvtpk(qa[qb][db][2] * SCL, qa[qb][db][3] * SCL)};
        const unsigned ch = 2u * (8u * kh + db) + (g >> 1);
        *(u32x2*)(smem + 3 * SLOT + (unsigned)(rg * 48 + qb * 16 + i16) * 512u + 16u * (ch ^ (unsigned)i16) + 8u * (g & 1)) = w;
      }
    asm volatile("s_waitcnt lgkmcnt(0)" ::: "memory");
    BARRIER();
#pragma unroll
    for (int qb = 0; qb < 3; ++qb)
#pragma unroll
      for (int ks = 0; ks < 8; ++ks)
        qf[qb][ks] = *(const bf16x8*)(smem + 3 * SLOT + (unsigned)(rg * 48 + qb * 16 + i16) * 512u + 16u * ((unsigned)(4 * ks + g) ^ (unsigned)i16));
  }

  f32x4 o[3][16];
  float mrun[3] = {0.f, 0.f, 0.f}, lsum[3], mtrue[3];
  f32x4 negm[3];
  int attempt = 0;
  int sl_c = 0, sl_i = 0;
  unsigned touch = 0;
  auto nxt = [](int x) { return x == NSLOT - 1 ? 0 : x + 1; };
  f32x4 sA[3][2], sB[3][2];
  bf16x8 pf[3];
#define ATT_PF 4
  constexpr int PF = ATT_PF;
  bf16x8 kpre[PF];
  bf16x4 vplo[PF], vphi[PF];

  constexpr unsigned WOS = 49152u;
  __amdgpu_buffer_rsrc_t rWo = __builtin_amdgcn_make_buffer_rsrc((void*)(Wo + (size_t)h * DH * DM), (short)0, DH * DM * 2, 0x00020000);
  unsigned wodo[7];
  auto issueWo = [&](int ks, int j) {
    dma_piece_w(ldsb + WOS + (unsigned)((ks + 2) & 3) * 28672u + (unsigned)(wid * 7 + j) * 1024u, wodo[j], rWo, (unsigned)ks * 28672u);
  };

  auto phaseA = [&](f32x4 (&sn)[3][2], f32x4 (&sp)[3][2], int tk, auto mode_tag) {
    constexpr bool FIRST = decltype(mode_tag)::value == 1;
    constexpr bool ISSUE = decltype(mode_tag)::value == 0;
    if constexpr (FIRST) STAMP(4); else STAMP(8);
    if constexpr (FIRST) WAIT_VM(8); else WAIT_VM(16);
    BARRIER();
    STAMP(5);
    const char* kb0 = smem + (unsigned)sl_c * SLOT;
    const char* nb0 = smem + (unsigned)nxt(sl_c) * SLOT;
    bf16x8 kf[16];
    unsigned pw[12];
    auto kread = [&](int i) { kf[i] = *(const bf16x8*)(kb0 + koff[(i >> 1) & 3] + (i >> 3) * 256 + (i & 1) * 8192); };
    if constexpr (FIRST) {
#pragma unroll
      for (int i = 0; i < PF; ++i) kread(i);
    } else {
#pragma unroll
      for (int i = 0; i < PF; ++i) kf[i] = kpre[i];
    }
    SCHED();
#pragma unroll
    for (int i = 0; i < 16; ++i) {
      const int ks = i >> 1, kb = i & 1;
#pragma unroll
      for (int qb = 0; qb < 3; ++qb) {
        const int m = 3 * i + qb;
        if (qb == 1) {
          if (i + PF < 16) kread(i + PF);
          else if constexpr (FIRST) kpre[i + PF - 16] = *(const bf16x8*)(nb0 + koff[((i + PF - 16) >> 1) & 3] + ((i + PF - 16) & 1) * 8192);
          else { const char* p = nb0 + voff[i + PF - 16]; vplo[i + PF - 16] = tr_read(p); vphi[i + PF - 16] = tr_read(p + 8192); }
        }
        if (qb == 2) {
          if constexpr (ISSUE) { FOR8(J, if (2 * J == i) issueK(tk, sl_i, IC(J));) }
          else if constexpr (FIRST) { FOR8(J, if (J == i) issueK(2, 3, IC(J)); if (J + 8 == i) issueV(1, 4, IC(J));) }
        }
        if constexpr (!FIRST) {
          const int e = m >> 1, eq = e >> 3, ej = e & 7;
          if ((m & 1) == 0) sp[eq][ej >> 2][ej & 3] = __builtin_amdgcn_exp2f(sp[eq][ej >> 2][ej & 3]);
          else if (e & 1) pw[e >> 1] = cvtpk(sp[eq][ej >> 2][(ej & 3) - 1], sp[eq][ej >> 2][ej & 3]);
        }
        if (ks == 0) { if constexpr (FIRST) mfma_v0(sn[qb][kb], kf[i], qf[qb][ks]); else mfma_vc(sn[qb][kb], kf[i], qf[qb][ks], negm[qb]); }
        else mfma_v(sn[qb][kb], kf[i], qf[qb][ks]);
        SCHED();
      }
    }
    if constexpr (!FIRST) {
#pragma unroll
      for (int qb = 0; qb < 3; ++qb) {
        u32x4 w = {pw[4 * qb], pw[4 * qb + 1], pw[4 * qb + 2], pw[4 * qb + 3]};
        pf[qb] = __builtin_bit_cast(bf16x8, w);
      }
      sl_i = nxt(sl_i);
    }
    sl_c = nxt(sl_c);
  };
  auto softmax_only = [&](f32x4 (&sp)[3][2]) {
#pragma unroll
    for (int qb = 0; qb < 3; ++qb) {
#pragma unroll
      for (int e = 0; e < 8; ++e) sp[qb][e >> 2][e & 3] = __builtin_amdgcn_exp2f(sp[qb][e >> 2][e & 3]);
      u32x4 w = {cvtpk(sp[qb][0][0], sp[qb][0][1]), cvtpk(sp[qb][0][2], sp[qb][0][3]), cvtpk(sp[qb][1][0], sp[qb][1][1]), cvtpk(sp[qb][1][2], sp[qb][1][3])};
      pf[qb] = __builtin_bit_cast(bf16x8, w);
    }
  };
  auto phaseB = [&](f32x4 (&sn)[3][2], f32x4 (&sp)[3][2], int tv, auto mode_tag) {
    constexpr int MODE = decltype(mode_tag)::value;
    STAMP(6);
    if constexpr (MODE == 2) WAIT_VM(14); else WAIT_VM(16);
    BARRIER();
    STAMP(7);
    const char* vb0 = smem + (unsigned)sl_c * SLOT;
    const char* nb0 = smem + (unsigned)nxt(sl_c) * SLOT;
    bf16x4 vlo[16], vhi[16];
    auto vread = [&](int db) {
      const char* p = vb0 + voff[db & 7] + (db >> 3) * 256;
      vlo[db] = tr_read(p); vhi[db] = tr_read(p + 8192);
    };
    if constexpr (MODE == 2) {
#pragma unroll
      for (int i = 0; i < PF; ++i) vread(i);
    } else {
#pragma unroll
      for (int i = 0; i < PF; ++i) { vlo[i] = vplo[i]; vhi[i] = vphi[i]; }
    }
    SCHED();
#pragma unroll
    for (int db = 0; db < 16; ++db) {
      bf16x8 vf = __builtin_shufflevector(vlo[db], vhi[db], 0, 1, 2, 3, 4, 5, 6, 7);
#pragma unroll
      for (int qb = 0; qb < 3; ++qb) {
        const int m = 3 * db + qb;
        if (qb == 1) {
          if (db + PF < 16) vread(db + PF);
          else if constexpr (MODE == 0) kpre[db + PF - 16] = *(const bf16x8*)(nb0 + koff[((db + PF - 16) >> 1) & 3] + ((db + PF - 16) & 1) * 8192);
        }
        if constexpr (MODE == 0) { if (qb == 2) { FOR8(J, if (2 * J == db) issueV(tv, sl_i, IC(J));) } }
        if constexpr (MODE == 1) { if (qb == 2 && db < 14) issueWo(db / 7, db % 7); }
        if ((m & 1) == 0) {
          const int e = m >> 1, eq = e >> 3, ej = e & 7;
          lsum[eq] = fadd_s(lsum[eq], sp[eq][ej >> 2][ej & 3]);
        } else if constexpr (MODE < 2) {
          const int e = m >> 1;
          if ((e & 1) == 0) {
            const int x = e >> 1, eq = x >> 2, ej = x & 3;
            mtrue[eq] = max3f(mtrue[eq], sn[eq][ej >> 1][(ej & 1) * 2], sn[eq][ej >> 1][(ej & 1) * 2 + 1]);
          }
        }
        mfma_acc(o[qb][db], vf, pf[qb]);
        SCHED();
      }
    }
    sl_c = nxt(sl_c); sl_i = nxt(sl_i);
  };
  using T_ = std::true_type;
  using F_ = std::false_type;
  char* myreg = smem + wid * 26624;
  char* preg = smem + (wid ^ 2) * 26624;

  for (;; ++attempt) {
    sl_c = 0; sl_i = 0;
#pragma unroll
    for (int qb = 0; qb < 3; ++qb) {
      lsum[qb] = 0.f;
#pragma unroll
      for (int db = 0; db < 16; ++db) o[qb][db] = (f32x4){0.f, 0.f, 0.f, 0.f};
    }
    phaseA(sA, sB, 0, IC(1));
    asm volatile("s_nop 7" ::: "memory");
    SCHED();
#pragma unroll
    for (int qb = 0; qb < 3; ++qb) {
      asm volatile("" : "+v"(sA[qb][0]), "+v"(sA[qb][1]));
      float r = max3f(max3f(sA[qb][0][0], sA[qb][0][1], sA[qb][0][2]), max3f(sA[qb][0][3], sA[qb][1][0], sA[qb][1][1]),
                      fmaxf(sA[qb][1][2], sA[qb][1][3]));
      if (attempt == 0) mrun[qb] = xmax_rows(r);
      mtrue[qb] = r - mrun[qb];
      negm[qb] = (f32x4){-mrun[qb], -mrun[qb], -mrun[qb], -mrun[qb]};
      sA[qb][0] += negm[qb]; sA[qb][1] += negm[qb];
    }
#pragma unroll 1
    for (int t = 0; t < 14; t += 2) {
      if (t == 6 && wid < 2) {
        const char* tp = (const char*)Wo + (size_t)h * DH * DM * 2 + (size_t)((b * 2 + rh) * 112 + wid * 64 + lane) * 128;
        if (wid * 64 + lane < 112) asm volatile("global_load_dword %0, %1, off" : "=v"(touch) : "v"(tp) : "memory");
      }
      phaseA(sB, sA, t + 3, IC(0));
      phaseB(sB, sA, t + 2, IC(0));
      phaseA(sA, sB, t + 4, IC(0));
      phaseB(sA, sB, t + 3, IC(0));
    }
    phaseA(sB, sA, 17, IC(2));
    {
      unsigned lz = lane;
      asm volatile("" : "+v"(lz));
#pragma unroll
      for (int j = 0; j < 7; ++j) {
        const unsigned n = (unsigned)(wid * 7 + j) * 64u + lz, r = n / 56u, c = n - r * 56u;
        wodo[j] = r * 896u + 16u * (c ^ (((r >> 1) & 3u) << 1));
      }
    }
    phaseB(sB, sA, 16, IC(1));
    softmax_only(sB);
    sl_c = nxt(sl_c);
    phaseB(sA, sB, 17, IC(2));

    asm volatile("" :: "v"(touch));
    STAMP(8);
    bool bad = false;
#pragma unroll
    for (int qb = 0; qb < 3; ++qb) {
      lsum[qb] = xsum_rows(lsum[qb]);
      mtrue[qb] = xmax_rows(mtrue[qb]);
      bad |= !(mtrue[qb] <= THR);
    }
    const int wbad = (attempt == 0 && __any(bad)) ? 1 : 0;
    asm volatile("s_nop 7\n\ts_nop 7" : PIN8A(o[0]), PIN8A(o[1]), PIN8A(o[2]));
    asm volatile("" : PIN8A(o[0] + 8), PIN8A(o[1] + 8), PIN8A(o[2] + 8));
    asm volatile("s_waitcnt lgkmcnt(0)" ::: "memory");
    BARRIER();
    if (kh) {
#pragma unroll
      for (int qb = 0; qb < 3; ++qb)
#pragma unroll
        for (int d = 0; d < 8; ++d) *(f32x4*)(myreg + (qb * 8 + d) * 1024 + lane * 16) = o[qb][d];
    } else {
#pragma unroll
      for (int qb = 0; qb < 3; ++qb)
#pragma unroll
        for (int d = 0; d < 8; ++d) *(f32x4*)(myreg + (qb * 8 + d) * 1024 + lane * 16) = o[qb][8 + d];
    }
#pragma unroll
    for (int qb = 0; qb < 3; ++qb) *(f32x2*)(myreg + 24576 + qb * 512 + lane * 8) = (f32x2){mrun[qb], lsum[qb]};
    if (lane == 0) *(int*)(myreg + 26112) = wbad;
    asm volatile("s_waitcnt lgkmcnt(0)" ::: "memory");
    BARRIER();
    int f = *(const int*)(smem + 26112) | *(const int*)(smem + 26624 + 26112) | *(const int*)(smem + 2 * 26624 + 26112) |
            *(const int*)(smem + 3 * 26624 + 26112);
    f = __builtin_amdgcn_readfirstlane(f);
    if (!f) break;
    asm volatile("s_waitcnt vmcnt(0) lgkmcnt(0)" ::: "memory");
    BARRIER();
#pragma unroll
    for (int qb = 0; qb < 3; ++qb) mrun[qb] += mtrue[qb];
    FOR8(J, issueK(0, 0, IC(J));)
    FOR8(J, issueK(1, 1, IC(J));)
    FOR8(J, issueV(0, 2, IC(J));)
  }

  STAMP(9);
  u32x4 ob[3][4];
  float fa0[3], fa1[3];
#pragma unroll
  for (int qb = 0; qb < 3; ++qb) {
    f32x2 ml = *(const f32x2*)(preg + 24576 + qb * 512 + lane * 8);
    float mn = fmaxf(mrun[qb], ml[0]);
    float a0 = __builtin_amdgcn_exp2f(mrun[qb] - mn), a1 = __builtin_amdgcn_exp2f(ml[0] - mn);
    float inv = 1.0f / (lsum[qb] * a0 + ml[1] * a1);
    fa0[qb] = a0 * inv; fa1[qb] = a1 * inv;
  }
  auto combine = [&](auto off_tag) {
    constexpr int OFF = decltype(off_tag)::value;
#pragma unroll
    for (int qb = 0; qb < 3; ++qb)
#pragma unroll
      for (int d = 0; d < 8; ++d) {
        f32x4 pv = *(const f32x4*)(preg + (qb * 8 + d) * 1024 + lane * 16);
        f32x4 r = o[qb][OFF + d] * fa0[qb] + pv * fa1[qb];
        ob[qb][d >> 1][2 * (d & 1)] = cvtpk(r[0], r[1]); ob[qb][d >> 1][2 * (d & 1) + 1] = cvtpk(r[2], r[3]);
      }
  };
  if (kh) combine(IC(8)); else combine(IC(0));
  asm volatile("s_waitcnt lgkmcnt(0)" ::: "memory");
  BARRIER();
#pragma unroll
  for (int qb = 0; qb < 3; ++qb)
#pragma unroll
    for (int dp = 0; dp < 4; ++dp)
      *(u32x4*)(smem + (unsigned)(rg * 48 + qb * 16 + i16) * 512u + 16u * ((unsigned)(kh * 16 + dp * 4 + g) ^ (unsigned)i16)) = ob[qb][dp];
#pragma unroll
  for (int j = 0; j < 7; ++j) issueWo(2, j);
#pragma unroll
  for (int j = 0; j < 7; ++j) issueWo(3, j);
  STAMP(10);

  {
    f32x4 pa[6][7];
#pragma unroll
    for (int mb = 0; mb < 6; ++mb)
#pragma unroll
      for (int nb = 0; nb < 7; ++nb) pa[mb][nb] = (f32x4){0.f, 0.f, 0.f, 0.f};
    unsigned wof[7];
    {
      const unsigned r = (unsigned)(4 * g + (i16 >> 2));
#pragma unroll
      for (int nb = 0; nb < 7; ++nb) {
        const unsigned seg = (unsigned)(wid * 7 + nb);
        wof[nb] = WOS + r * 896u + 32u * (seg ^ ((r >> 1) & 3u)) + 8u * (unsigned)(i16 & 3);
      }
    }
    bf16x8 of[6], ofn[6];
    bf16x4 wlo[7], whi[7], wplo[3], wphi[3];
    STAMP(11);
    WAIT_VM(14);
    BARRIER();
    STAMP(12);
    {
      const char* ab = smem + (unsigned)i16 * 512u + 16u * ((unsigned)g ^ (unsigned)i16);
#pragma unroll
      for (int mb = 0; mb < 6; ++mb) ofn[mb] = *(const bf16x8*)(ab + mb * 8192);
#pragma unroll
      for (int nb = 0; nb < 3; ++nb) { wplo[nb] = tr_read(smem + 2 * 28672 + wof[nb]); wphi[nb] = tr_read(smem + 2 * 28672 + wof[nb] + 16 * 896); }
    }
#pragma unroll 1
    for (int ks = 0; ks < 8; ++ks) {
      STAMP(13);
      if (ks > 0) { WAIT_VM(7); BARRIER(); }
      STAMP(12);
      const char* wb = smem + (unsigned)((ks + 2) & 3) * 28672u;
      const char* wn = smem + (unsigned)((ks + 3) & 3) * 28672u;
      const char* an = smem + (unsigned)i16 * 512u + 16u * ((unsigned)(4 * ((ks + 1) & 7) + g) ^ (unsigned)i16);
#pragma unroll
      for (int mb = 0; mb < 6; ++mb) of[mb] = ofn[mb];
#pragma unroll
      for (int nb = 0; nb < 3; ++nb) { wlo[nb] = wplo[nb]; whi[nb] = wphi[nb]; }
      SCHED();
#pragma unroll
      for (int nb = 0; nb < 7; ++nb) {
        bf16x8 wf = __builtin_shufflevector(wlo[nb], whi[nb], 0, 1, 2, 3, 4, 5, 6, 7);
#pragma unroll
        for (int mb = 0; mb < 6; ++mb) {
          if (mb == 1) {
            if (nb + 3 < 7) { wlo[nb + 3] = tr_read(wb + wof[nb + 3]); whi[nb + 3] = tr_read(wb + wof[nb + 3] + 16 * 896); }
            else { wplo[nb - 4] = tr_read(wn + wof[nb - 4]); wphi[nb - 4] = tr_read(wn + wof[nb - 4] + 16 * 896); }
          }
          if (mb == 3 && ks > 0) issueWo(ks + 3, nb);
          if (mb == 5 && nb < 6) ofn[nb] = *(const bf16x8*)(an + nb * 8192);
          mfma_acc(pa[mb][nb], wf, of[mb]);
          SCHED();
        }
      }
    }
    STAMP(13);
    asm volatile("s_nop 7\n\ts_nop 7" : PIN7A(pa[0]), PIN7A(pa[1]), PIN7A(pa[2]));
    asm volatile("" : PIN7A(pa[3]), PIN7A(pa[4]), PIN7A(pa[5]));
    u16* pp = Part + ((size_t)(h * 16 + b * 2 + rh) * 4 + wid) * (21 * 512) + lane * 8;
#pragma unroll
    for (int mp = 0; mp < 3; ++mp)
#pragma unroll
      for (int nb = 0; nb < 7; ++nb) {
        const f32x4 x0 = pa[2 * mp][nb], x1 = pa[2 * mp + 1][nb];
        u32x4 w = {cvtpk(x0[0], x0[1]), cvtpk(x0[2], x0[3]), cvtpk(x1[0], x1[1]), cvtpk(x1[2], x1[3])};
        *(u32x4*)(pp + (mp * 7 + nb) * 512) = w;
      }
  }
  STAMP(14);
  STAMP_END();
}

__global__ __launch_bounds__(256) void reduce_kernel(const u16* __restrict__ Part, u16* __restrict__ out) {
  const int gw = blockIdx.x * 4 + (threadIdx.x >> 6), lane = threadIdx.x & 63;
  const u16* p = Part + (size_t)gw * 512 + lane * 8;
  u32x4 v[16];
#pragma unroll
  for (int h = 0; h < 16; ++h) v[h] = __builtin_nontemporal_load((const u32x4*)(p + (size_t)h * (16 * 4 * 21 * 512)));
  float a[8] = {0.f, 0.f, 0.f, 0.f, 0.f, 0.f, 0.f, 0.f};
#pragma unroll
  for (int h = 0; h < 16; ++h)
#pragma unroll
    for (int e = 0; e < 4; ++e) {
      a[2 * e] += __uint_as_float(v[h][e] << 16);
      a[2 * e + 1] += __uint_as_float(v[h][e] & 0xffff0000u);
    }
  const int blk = gw % 21, rw = gw / 21, w = rw & 3, rt = rw >> 2, mp = blk / 7, nb = blk - mp * 7;
  u16* op = out + (size_t)(rt * 96 + mp * 32 + (lane & 15)) * DM + w * 112 + nb * 16 + (lane >> 4) * 4;
  *(u32x2*)op = (u32x2){cvtpk(a[0], a[1]), cvtpk(a[2], a[3])};
  *(u32x2*)(op + 16 * DM) = (u32x2){cvtpk(a[4], a[5]), cvtpk(a[6], a[7])};
}

extern "C" void kernel_launch(void* const* d_in, const int* in_sizes, int n_in, void* d_out, int out_size,
                              void* d_ws, size_t ws_size, hipStream_t stream) {
  const u16* x = (const u16*)d_in[0];
  const u16* kc = (const u16*)d_in[1];
  const u16* vc = (const u16*)d_in[2];
  const u16* wq = (const u16*)d_in[3];
  const u16* wo = (const u16*)d_in[4];
  u16* out = (u16*)d_out;
  u16* part = (u16*)d_ws;
  (void)hipFuncSetAttribute((const void*)attn_kernel, hipFuncAttributeMaxDynamicSharedMemorySize, ATT_LDS);
  attn_kernel<<<256, 256, ATT_LDS, stream>>>(x, wq, kc, vc, wo, part);
  reduce_kernel<<<336, 256, 0, stream>>>(part, out);
}
```

```cpp
#include <hip/hip_runtime.h>
#include <hip/hip_bf16.h>

typedef unsigned short u16;
typedef __attribute__((ext_vector_type(8))) short bf16x8;
typedef __attribute__((ext_vector_type(4))) short bf16x4;
typedef __attribute__((ext_vector_type(4))) float f32x4;
typedef __attribute__((ext_vector_type(2))) float f32x2;
typedef __attribute__((ext_vector_type(2))) __bf16 bf16x2_t;
typedef __attribute__((ext_vector_type(4))) unsigned u32x4;
typedef __attribute__((ext_vector_type(2))) unsigned u32x2;
typedef __attribute__((address_space(3))) bf16x4* lds_b64_ptr;

#define DI __device__ __forceinline__

constexpr int NB = 8, SQ = 192, DM = 448, NH = 16, SK = 1024, DH = 256;
constexpr int HD = NH * DH;

DI unsigned cvtpk(float lo, float hi) {
  f32x2 v = {lo, hi};
  bf16x2_t b = __builtin_convertvector(v, bf16x2_t);
  return __builtin_bit_cast(unsigned, b);
}
DI bf16x4 tr_read(const char* p) {
  return __builtin_amdgcn_ds_read_tr16_b64_v4i16((lds_b64_ptr)(p));
}

constexpr int SLOT = 32768;
constexpr int NSLOT = 5;
constexpr int ATT_LDS = SLOT * NSLOT;
constexpr float SCL = 0.0625f * 1.4426950408889634f;
constexpr float THR = 40.0f;

#define WAIT_VM(n) asm volatile("s_waitcnt vmcnt(" #n ") lgkmcnt(0)" ::: "memory")
#define BARRIER() __builtin_amdgcn_s_barrier()

#define SCHED() __builtin_amdgcn_sched_barrier(0)
#define STAMPL(k)
#define STAMP_END2()
#define STAMP(k)
#define STAMP_INIT()
#define STAMP_END()
DI void mfma_acc(f32x4& d, bf16x8 a, bf16x8 b) {
  asm volatile("v_mfma_f32_16x16x32_bf16 %0, %1, %2, %0" : "+a"(d) : "v"(a), "v"(b));
}
DI void mfma_v(f32x4& d, bf16x8 a, bf16x8 b) {
  asm volatile("v_mfma_f32_16x16x32_bf16 %0, %1, %2, %0" : "+v"(d) : "v"(a), "v"(b));
}
DI void mfma_v0(f32x4& d, bf16x8 a, bf16x8 b) {
  asm volatile("v_mfma_f32_16x16x32_bf16 %0, %1, %2, 0" : "=&v"(d) : "v"(a), "v"(b));
}

DI void mfma_acc0(f32x4& d, bf16x8 a, bf16x8 b) {
  asm volatile("v_mfma_f32_16x16x32_bf16 %0, %1, %2, 0" : "=a"(d) : "v"(a), "v"(b));
}
DI void mfma_vc(f32x4& d, bf16x8 a, bf16x8 b, f32x4 c) {
  asm volatile("v_mfma_f32_16x16x32_bf16 %0, %1, %2, %3" : "=&v"(d) : "v"(a), "v"(b), "v"(c));
}
#define PIN8A(x) "+a"((x)[0]), "+a"((x)[1]), "+a"((x)[2]), "+a"((x)[3]), "+a"((x)[4]), "+a"((x)[5]), "+a"((x)[6]), "+a"((x)[7])
#define PIN7A(x) "+a"((x)[0]), "+a"((x)[1]), "+a"((x)[2]), "+a"((x)[3]), "+a"((x)[4]), "+a"((x)[5]), "+a"((x)[6])
DI float max3f(float a, float b, float c) { float r; asm("v_max3_f32 %0, %1, %2, %3" : "=v"(r) : "v"(a), "v"(b), "v"(c)); return r; }
DI float fadd_s(float a, float b) { float r; asm("v_add_f32_e32 %0, %1, %2" : "=v"(r) : "v"(a), "v"(b)); return r; }

template <int OFF>
DI void dma_piece(unsigned ldsdst, unsigned voff, __amdgpu_buffer_rsrc_t rsrc, unsigned soff) {
  asm volatile("s_mov_b32 m0, %0\n\ts_nop 0\n\tbuffer_load_dwordx4 %1, %2, %3 offen offset:%4 nt lds"
               :: "s"(ldsdst), "v"(voff), "s"(rsrc), "s"(soff), "i"(OFF) : "memory");
}
DI void dma_piece_w(unsigned ldsdst, unsigned voff, __amdgpu_buffer_rsrc_t rsrc, unsigned soff) {
  asm volatile("s_mov_b32 m0, %0\n\ts_nop 0\n\tbuffer_load_dwordx4 %1, %2, %3 offen lds"
               :: "s"(ldsdst), "v"(voff), "s"(rsrc), "s"(soff) : "memory");
}
DI float xmax_rows(float v) {
  auto a = __builtin_amdgcn_permlane16_swap(__float_as_uint(v), __float_as_uint(v), false, false);
  v = fmaxf(__uint_as_float(a[0]), __uint_as_float(a[1]));
  auto c = __builtin_amdgcn_permlane32_swap(__float_as_uint(v), __float_as_uint(v), false, false);
  return fmaxf(__uint_as_float(c[0]), __uint_as_float(c[1]));
}
DI float xsum_rows(float v) {
  auto a = __builtin_amdgcn_permlane16_swap(__float_as_uint(v), __float_as_uint(v), false, false);
  v = __uint_as_float(a[0]) + __uint_as_float(a[1]);
  auto c = __builtin_amdgcn_permlane32_swap(__float_as_uint(v), __float_as_uint(v), false, false);
  return __uint_as_float(c[0]) + __uint_as_float(c[1]);
}

__global__ __launch_bounds__(256, 1) void attn_kernel(const u16* __restrict__ X, const u16* __restrict__ Wq,
                                                      const u16* __restrict__ Kc, const u16* __restrict__ Vc,
                                                      const u16* __restrict__ Wo, u16* __restrict__ Part) {
  extern __shared__ __attribute__((aligned(16))) char smem[];
  STAMP_INIT();
  const int tid = threadIdx.x, lane = tid & 63;
  const int wid = __builtin_amdgcn_readfirstlane(tid >> 6);
  const int rg = wid & 1, kh = wid >> 1;
  const int bid = blockIdx.x;
  const int idx = bid >> 3, rh = idx & 1, h = 2 * (bid & 7) + ((idx >> 1) & 1), b = idx >> 2;
  const int g = lane >> 4, i16 = lane & 15;
  const unsigned ldsb = (unsigned)(size_t)smem;
  const int q0 = rh * 96 + rg * 48;
  unsigned ctouch = 0;
  {
    unsigned long long pc; unsigned csz;
    asm volatile("s_getpc_b64 %0\n.Lattn_code_begin:\n\ts_mov_b32 %1, .Lall_code_end-.Lattn_code_begin" : "=s"(pc), "=s"(csz));
    const unsigned off = (unsigned)tid * 128u + (unsigned)(idx & 1) * 32768u;
    if (csz <= 65536u && off < csz) asm volatile("global_load_dword %0, %1, off" : "=v"(ctouch) : "v"(pc + off) : "memory");
  }

  const char* Kbase = (const char*)Kc + (size_t)(b * NH + h) * SK * DH * 2;
  const char* Vbase = (const char*)Vc + (size_t)(b * NH + h) * SK * DH * 2;
  __amdgpu_buffer_rsrc_t rK = __builtin_amdgcn_make_buffer_rsrc((void*)Kbase, (short)0, SK * DH * 2, 0x00020000);
  __amdgpu_buffer_rsrc_t rV = __builtin_amdgcn_make_buffer_rsrc((void*)Vbase, (short)0, SK * DH * 2, 0x00020000);
  __amdgpu_buffer_rsrc_t rW = __builtin_amdgcn_make_buffer_rsrc((void*)Wq, (short)0, DM * HD * 2, 0x00020000);
  const unsigned wsoff = (unsigned)wid * 8192u;
  unsigned kdo[8], vdo[4], wdo[4];
  {
    const unsigned hi = lane >> 5, l32 = lane & 31;
#pragma unroll
    for (int j = 0; j < 8; ++j) kdo[j] = hi * 512u + 16u * (l32 ^ (2u * j + hi));
#pragma unroll
    for (int j = 0; j < 4; ++j) vdo[j] = hi * 512u + 16u * (l32 ^ ((2u * j + hi) << 1));
#pragma unroll
    for (int j = 0; j < 4; ++j) wdo[j] = hi * 8192u + 16u * (l32 ^ (((2u * (j & 1) + hi) | ((j >> 1) << 2)) << 1));
  }
  auto issueK = [&](int tl, int slot, auto jt) {
    constexpr int j = decltype(jt)::value;
    dma_piece<(j & 3) * 1024>(ldsb + (unsigned)slot * SLOT + wsoff + 4096u * (j >> 2), kdo[j], rK,
                              (unsigned)tl * 32768u + wsoff + 4096u * (j >> 2));
  };
  auto issueV = [&](int tl, int slot, auto jt) {
    constexpr int j = decltype(jt)::value;
    dma_piece<(j & 3) * 1024>(ldsb + (unsigned)slot * SLOT + wsoff + 4096u * (j >> 2), vdo[j & 3], rV,
                              (unsigned)tl * 32768u + wsoff + 4096u * (j >> 2));
  };
  auto issueW = [&](int c, int slot, int j) {
    dma_piece_w(ldsb + (unsigned)slot * SLOT + wsoff + 1024u * j, wdo[(j & 1) + 2 * (j >> 2)], rW,
                (unsigned)(64 * c + 16 * wid + 2 * j) * 8192u + (unsigned)h * 512u);
  };
#define FOR8(J, ...) { { constexpr int J = 0; __VA_ARGS__ } { constexpr int J = 1; __VA_ARGS__ } { constexpr int J = 2; __VA_ARGS__ } { constexpr int J = 3; __VA_ARGS__ } \
                       { constexpr int J = 4; __VA_ARGS__ } { constexpr int J = 5; __VA_ARGS__ } { constexpr int J = 6; __VA_ARGS__ } { constexpr int J = 7; __VA_ARGS__ } }
#define IC(x) std::integral_constant<int, (x)>{}

  {
    __amdgpu_buffer_rsrc_t rX = __builtin_amdgcn_make_buffer_rsrc((void*)(X + (size_t)(b * SQ + rh * 96) * DM), (short)0, 96 * DM * 2, 0x00020000);
#pragma unroll
    for (int j = 0; j < 21; ++j) {
      const unsigned n = (unsigned)(wid * 21 + j) * 64u + lane, r = n / 56u, c = n - r * 56u;
      dma_piece_w(ldsb + (unsigned)(wid * 21 + j) * 1024u, r * 896u + 16u * (c ^ ((r >> 1) & 7u)), rX, 0u);
    }
  }
#pragma unroll
  for (int c = 0; c < 2; ++c)
#pragma unroll
    for (int j = 0; j < 8; ++j) issueW(c, c + 3, j);

  unsigned koff[4], voff[8];
#pragma unroll
  for (int a = 0; a < 4; ++a) koff[a] = (unsigned)(kh * 32 + i16) * 512u + 16u * (unsigned)((4 * a + g) ^ i16);
  {
    const int row = 4 * g + (i16 >> 2), r7 = row & 7;
#pragma unroll
    for (int bb = 0; bb < 8; ++bb) voff[bb] = (unsigned)(kh * 32 + row) * 512u + 32u * (unsigned)(bb ^ r7) + 8u * (unsigned)(i16 & 3);
  }

  bf16x8 qf[3][8];
  {
    f32x4 qa[3][8];
    unsigned woff[8];
    {
      const int q4 = i16 >> 2, f = q4 | ((g & 1) << 2);
#pragma unroll
      for (int db = 0; db < 8; ++db) woff[db] = (unsigned)(8 * g + q4) * 512u + (unsigned)kh * 256u + 32u * (unsigned)(db ^ f) + 8u * (unsigned)(i16 & 3);
    }
    bf16x8 xf[3][14];
    STAMP(0);
    WAIT_VM(16);
    BARRIER();
    STAMP(1);
#pragma unroll
    for (int qb = 0; qb < 3; ++qb) {
      const unsigned r = (unsigned)(rg * 48 + qb * 16 + i16);
#pragma unroll
      for (int kk = 0; kk < 14; ++kk)
        xf[qb][kk] = *(const bf16x8*)(smem + r * 896u + 128u * (kk >> 1) + 16u * ((unsigned)(4 * (kk & 1) + g) ^ ((r >> 1) & 7u)));
    }
    bf16x4 wplo[4], wphi[4];
    auto qstep = [&](auto ctag) {
      constexpr int c = decltype(ctag)::value;
      STAMPL(2);
      if (c == 0) WAIT_VM(0); else WAIT_VM(16);
      BARRIER();
      STAMPL(3);
      const char* wb0 = smem + ((c + 3) % 5) * SLOT;
      const char* wn0 = smem + ((c + 4) % 5) * SLOT;
      constexpr int PF = 4;
      bf16x4 wlo[16], whi[16];
      auto wread = [&](int i) {
        const char* p = wb0 + woff[i & 7] + (i >> 3) * 16384;
        wlo[i] = tr_read(p); whi[i] = tr_read(p + 2048);
      };
#pragma unroll
      for (int i = 0; i < PF; ++i) { if (c == 0) wread(i); else { wlo[i] = wplo[i]; whi[i] = wphi[i]; } }
      SCHED();
#pragma unroll
      for (int i = 0; i < 16; ++i) {
        bf16x8 wf = __builtin_shufflevector(wlo[i], whi[i], 0, 1, 2, 3, 4, 5, 6, 7);
#pragma unroll
        for (int qb = 0; qb < 3; ++qb) {
          if (qb == 0) {
            if (i + PF < 16) wread(i + PF);
            else if (c < 6) { wplo[i + PF - 16] = tr_read(wn0 + woff[i + PF - 16]); wphi[i + PF - 16] = tr_read(wn0 + woff[i + PF - 16] + 2048); }
          }
          if (c == 0) {
            if (qb == 1) { if (i < 8) issueW(2, 0, i); else issueW(4, 2, i - 8); }
            if (qb == 2 && i < 8) issueW(3, 1, i);
          } else if (qb == 2 && (i & 1)) {
            if (c < 3) issueW(c + 4, (c + 7) % 5, i >> 1);
            FOR8(J, if (J == (i >> 1)) { if (c == 3) issueK(0, 0, IC(J)); if (c == 4) issueK(1, 1, IC(J)); if (c == 5) issueV(0, 2, IC(J)); })
          }
          if (c == 0 && i < 8) mfma_acc0(qa[qb][i & 7], wf, xf[qb][0]);
          else mfma_acc(qa[qb][i & 7], wf, xf[qb][2 * c + (i >> 3)]);
          SCHED();
        }
      }
    };
    qstep(IC(0)); qstep(IC(1)); qstep(IC(2)); qstep(IC(3)); qstep(IC(4)); qstep(IC(5)); qstep(IC(6));
    STAMP(2);
    asm volatile("s_nop 7\n\ts_nop 7" : PIN8A(qa[0]), PIN8A(qa[1]), PIN8A(qa[2]));
    asm volatile("s_waitcnt lgkmcnt(0)" ::: "memory");
    BARRIER();
#pragma unroll
    for (int qb = 0; qb < 3; ++qb)
#pragma unroll
      for (int db = 0; db < 8; ++db) {
        u32x2 w = {cvtpk(qa[qb][db][0] * SCL, qa[qb][db][1] * SCL), cvtpk(qa[qb][db][2] * SCL, qa[qb][db][3] * SCL)};
        const unsigned ch = 2u * (8u * kh + db) + (g >> 1);
        *(u32x2*)(smem + 3 * SLOT + (unsigned)(rg * 48 + qb * 16 + i16) * 512u + 16u * (ch ^ (unsigned)i16) + 8u * (g & 1)) = w;
      }
    asm volatile("s_waitcnt lgkmcnt(0)" ::: "memory");
    BARRIER();
#pragma unroll
    for (int qb = 0; qb < 3; ++qb)
#pragma unroll
      for (int ks = 0; ks < 8; ++ks)
        qf[qb][ks] = *(const bf16x8*)(smem + 3 * SLOT + (unsigned)(rg * 48 + qb * 16 + i16) * 512u + 16u * ((unsigned)(4 * ks + g) ^ (unsigned)i16));
  }

  f32x4 o[3][16];
  float mrun[3] = {0.f, 0.f, 0.f}, lsum[3], mtrue[3];
  f32x4 negm[3];
  int attempt = 0;
  int sl_c = 0, sl_i = 0;
  unsigned touch = 0;
  auto nxt = [](int x) { return x == NSLOT - 1 ? 0 : x + 1; };
  f32x4 sA[3][2], sB[3][2];
  bf16x8 pf[3];
#define ATT_PF 4
  constexpr int PF = ATT_PF;
  bf16x8 kpre[PF];
  bf16x4 vplo[PF], vphi[PF];

  constexpr unsigned WOS = 49152u;
  __amdgpu_buffer_rsrc_t rWo = __builtin_amdgcn_make_buffer_rsrc((void*)(Wo + (size_t)h * DH * DM), (short)0, DH * DM * 2, 0x00020000);
  unsigned wodo[7];
  auto issueWo = [&](int ks, int j) {
    dma_piece_w(ldsb + WOS + (unsigned)((ks + 2) & 3) * 28672u + (unsigned)(wid * 7 + j) * 1024u, wodo[j], rWo, (unsigned)ks * 28672u);
  };

  auto phaseA = [&](f32x4 (&sn)[3][2], f32x4 (&sp)[3][2], int tk, auto mode_tag) {
    constexpr bool FIRST = decltype(mode_tag)::value == 1;
    constexpr bool ISSUE = decltype(mode_tag)::value == 0;
    if constexpr (FIRST) STAMP(4); else STAMPL(8);
    if constexpr (FIRST) WAIT_VM(8); else WAIT_VM(16);
    BARRIER();
    STAMPL(5);
    const char* kb0 = smem + (unsigned)sl_c * SLOT;
    const char* nb0 = smem + (unsigned)nxt(sl_c) * SLOT;
    bf16x8 kf[16];
    unsigned pw[12];
    auto kread = [&](int i) { kf[i] = *(const bf16x8*)(kb0 + koff[(i >> 1) & 3] + (i >> 3) * 256 + (i & 1) * 8192); };
    if constexpr (FIRST) {
#pragma unroll
      for (int i = 0; i < PF; ++i) kread(i);
    } else {
#pragma unroll
      for (int i = 0; i < PF; ++i) kf[i] = kpre[i];
    }
    SCHED();
#pragma unroll
    for (int i = 0; i < 16; ++i) {
      const int ks = i >> 1, kb = i & 1;
#pragma unroll
      for (int qb = 0; qb < 3; ++qb) {
        const int m = 3 * i + qb, u = m % 6, blk = m / 6;
        if (qb == 0) {
          if (i + PF < 16) kread(i + PF);
          else if constexpr (FIRST) kpre[i + PF - 16] = *(const bf16x8*)(nb0 + koff[((i + PF - 16) >> 1) & 3] + ((i + PF - 16) & 1) * 8192);
          else { const char* p = nb0 + voff[i + PF - 16]; vplo[i + PF - 16] = tr_read(p); vphi[i + PF - 16] = tr_read(p + 8192); }
          if constexpr (!FIRST) {
            const int done = 3 * blk + (u == 3 ? 2 : 0), prev = (u == 3) ? 3 * blk : (blk > 0 ? 3 * (blk - 1) + 2 : 0);
#pragma unroll
            for (int k = 0; k < 12; ++k)
              if (2 * k + 2 <= done && 2 * k + 2 > prev) pw[k] = cvtpk(sp[k >> 2][(k >> 1) & 1][2 * (k & 1)], sp[k >> 2][(k >> 1) & 1][2 * (k & 1) + 1]);
          }
        }
        if (u == 5) {
          if constexpr (ISSUE) { FOR8(J, if (J == blk) issueK(tk, sl_i, IC(J));) }
        }
        if constexpr (FIRST) { if (qb == 2) { FOR8(J, if (J == i) issueK(2, 3, IC(J)); if (J + 8 == i) issueV(1, 4, IC(J));) } }
        if constexpr (!FIRST) {
          if (u == 1 || u == 2 || u == 4) {
            const int e = 3 * blk + (u == 1 ? 0 : u == 2 ? 1 : 2), eq = e >> 3, ej = e & 7;
            sp[eq][ej >> 2][ej & 3] = __builtin_amdgcn_exp2f(sp[eq][ej >> 2][ej & 3]);
          }
        }
        if (ks == 0) { if constexpr (FIRST) mfma_v0(sn[qb][kb], kf[i], qf[qb][ks]); else mfma_vc(sn[qb][kb], kf[i], qf[qb][ks], negm[qb]); }
        else mfma_v(sn[qb][kb], kf[i], qf[qb][ks]);
        SCHED();
      }
    }
    if constexpr (!FIRST) {
      pw[11] = cvtpk(sp[2][1][2], sp[2][1][3]);
#pragma unroll
      for (int qb = 0; qb < 3; ++qb) {
        u32x4 w = {pw[4 * qb], pw[4 * qb + 1], pw[4 * qb + 2], pw[4 * qb + 3]};
        pf[qb] = __builtin_bit_cast(bf16x8, w);
      }
      sl_i = nxt(sl_i);
    }
    sl_c = nxt(sl_c);
  };
  auto softmax_only = [&](f32x4 (&sp)[3][2]) {
#pragma unroll
    for (int qb = 0; qb < 3; ++qb) {
#pragma unroll
      for (int e = 0; e < 8; ++e) sp[qb][e >> 2][e & 3] = __builtin_amdgcn_exp2f(sp[qb][e >> 2][e & 3]);
      u32x4 w = {cvtpk(sp[qb][0][0], sp[qb][0][1]), cvtpk(sp[qb][0][2], sp[qb][0][3]), cvtpk(sp[qb][1][0], sp[qb][1][1]), cvtpk(sp[qb][1][2], sp[qb][1][3])};
      pf[qb] = __builtin_bit_cast(bf16x8, w);
    }
  };
  auto phaseB = [&](f32x4 (&sn)[3][2], f32x4 (&sp)[3][2], int tv, auto mode_tag) {
    constexpr int MODE = decltype(mode_tag)::value;
    STAMPL(6);
    if constexpr (MODE == 2) WAIT_VM(14); else WAIT_VM(16);
    BARRIER();
    STAMPL(7);
    const char* vb0 = smem + (unsigned)sl_c * SLOT;
    const char* nb0 = smem + (unsigned)nxt(sl_c) * SLOT;
    bf16x4 vlo[16], vhi[16];
    auto vread = [&](int db) {
      const char* p = vb0 + voff[db & 7] + (db >> 3) * 256;
      vlo[db] = tr_read(p); vhi[db] = tr_read(p + 8192);
    };
    if constexpr (MODE == 2) {
#pragma unroll
      for (int i = 0; i < PF; ++i) vread(i);
    } else {
#pragma unroll
      for (int i = 0; i < PF; ++i) { vlo[i] = vplo[i]; vhi[i] = vphi[i]; }
    }
    SCHED();
#pragma unroll
    for (int db = 0; db < 16; ++db) {
      bf16x8 vf = __builtin_shufflevector(vlo[db], vhi[db], 0, 1, 2, 3, 4, 5, 6, 7);
#pragma unroll
      for (int qb = 0; qb < 3; ++qb) {
        const int m = 3 * db + qb, u = m % 6, blk = m / 6;
        if (qb == 0) {
          if (db + PF < 16) vread(db + PF);
          else if constexpr (MODE == 0) kpre[db + PF - 16] = *(const bf16x8*)(nb0 + koff[((db + PF - 16) >> 1) & 3] + ((db + PF - 16) & 1) * 8192);
        }
        if constexpr (MODE == 0) { if (u == 5) { FOR8(J, if (J == blk) issueV(tv, sl_i, IC(J));) } }
        if constexpr (MODE == 1) { if (qb == 2 && db < 14) issueWo(db / 7, db % 7); }
        if (u == 1 || u == 2) {
#pragma unroll
          for (int z = 0; z < (u == 1 ? 2 : 1); ++z) {
            const int e = 3 * blk + (u == 1 ? z : 2), eq = e >> 3, ej = e & 7;
            lsum[eq] = fadd_s(lsum[eq], sp[eq][ej >> 2][ej & 3]);
          }
        }
        if constexpr (MODE < 2) {
          if (u == 2 || (u == 4 && (blk & 1) == 0)) {
            const int x = (3 * blk) / 2 + (u == 4 ? 1 : ((blk & 1) ? 1 : 0)) , eq = x >> 2, ej = x & 3;
            mtrue[eq] = max3f(mtrue[eq], sn[eq][ej >> 1][(ej & 1) * 2], sn[eq][ej >> 1][(ej & 1) * 2 + 1]);
          }
        }
        mfma_acc(o[qb][db], vf, pf[qb]);
        SCHED();
      }
    }
    sl_c = nxt(sl_c); sl_i = nxt(sl_i);
  };
  using T_ = std::true_type;
  using F_ = std::false_type;
  char* myreg = smem + wid * 26624;
  char* preg = smem + (wid ^ 2) * 26624;

  for (;; ++attempt) {
    sl_c = 0; sl_i = 0;
#pragma unroll
    for (int qb = 0; qb < 3; ++qb) {
      lsum[qb] = 0.f;
#pragma unroll
      for (int db = 0; db < 16; ++db) o[qb][db] = (f32x4){0.f, 0.f, 0.f, 0.f};
    }
    asm volatile("" : PIN8A(o[0]), PIN8A(o[1]), PIN8A(o[2]));
    asm volatile("" : PIN8A(o[0] + 8), PIN8A(o[1] + 8), PIN8A(o[2] + 8));
    phaseA(sA, sB, 0, IC(1));
    asm volatile("s_nop 7" ::: "memory");
    SCHED();
#pragma unroll
    for (int qb = 0; qb < 3; ++qb) {
      asm volatile("" : "+v"(sA[qb][0]), "+v"(sA[qb][1]));
      float r = max3f(max3f(sA[qb][0][0], sA[qb][0][1], sA[qb][0][2]), max3f(sA[qb][0][3], sA[qb][1][0], sA[qb][1][1]),
                      fmaxf(sA[qb][1][2], sA[qb][1][3]));
      if (attempt == 0) mrun[qb] = xmax_rows(r);
      mtrue[qb] = r - mrun[qb];
      negm[qb] = (f32x4){-mrun[qb], -mrun[qb], -mrun[qb], -mrun[qb]};
      sA[qb][0] += negm[qb]; sA[qb][1] += negm[qb];
    }
#pragma unroll 1
    for (int t = 0; t < 14; t += 2) {
      if (t == 6 && wid < 2) {
        const char* tp = (const char*)Wo + (size_t)h * DH * DM * 2 + (size_t)((b * 2 + rh) * 112 + wid * 64 + lane) * 128;
        if (wid * 64 + lane < 112) asm volatile("global_load_dword %0, %1, off" : "=v"(touch) : "v"(tp) : "memory");
      }
      phaseA(sB, sA, t + 3, IC(0));
      phaseB(sB, sA, t + 2, IC(0));
      phaseA(sA, sB, t + 4, IC(0));
      phaseB(sA, sB, t + 3, IC(0));
    }
    phaseA(sB, sA, 17, IC(2));
    {
      unsigned lz = lane;
      asm volatile("" : "+v"(lz));
#pragma unroll
      for (int j = 0; j < 7; ++j) {
        const unsigned n = (unsigned)(wid * 7 + j) * 64u + lz, r = n / 56u, c = n - r * 56u;
        wodo[j] = r * 896u + 16u * (c ^ (((r >> 1) & 3u) << 1));
      }
    }
    phaseB(sB, sA, 16, IC(1));
    softmax_only(sB);
    sl_c = nxt(sl_c);
    phaseB(sA, sB, 17, IC(2));

    asm volatile("" :: "v"(touch), "v"(ctouch));
    STAMP(8);
    bool bad = false;
#pragma unroll
    for (int qb = 0; qb < 3; ++qb) {
      lsum[qb] = xsum_rows(lsum[qb]);
      mtrue[qb] = xmax_rows(mtrue[qb]);
      bad |= !(mtrue[qb] <= THR);
    }
    const int wbad = (attempt == 0 && __any(bad)) ? 1 : 0;
    asm volatile("s_nop 7\n\ts_nop 7" : PIN8A(o[0]), PIN8A(o[1]), PIN8A(o[2]));
    asm volatile("" : PIN8A(o[0] + 8), PIN8A(o[1] + 8), PIN8A(o[2] + 8));
    asm volatile("s_waitcnt lgkmcnt(0)" ::: "memory");
    BARRIER();
    if (kh) {
#pragma unroll
      for (int qb = 0; qb < 3; ++qb)
#pragma unroll
        for (int d = 0; d < 8; ++d) *(f32x4*)(myreg + (qb * 8 + d) * 1024 + lane * 16) = o[qb][d];
    } else {
#pragma unroll
      for (int qb = 0; qb < 3; ++qb)
#pragma unroll
        for (int d = 0; d < 8; ++d) *(f32x4*)(myreg + (qb * 8 + d) * 1024 + lane * 16) = o[qb][8 + d];
    }
#pragma unroll
    for (int qb = 0; qb < 3; ++qb) *(f32x2*)(myreg + 24576 + qb * 512 + lane * 8) = (f32x2){mrun[qb], lsum[qb]};
    if (lane == 0) *(int*)(myreg + 26112) = wbad;
    asm volatile("s_waitcnt lgkmcnt(0)" ::: "memory");
    BARRIER();
    int f = *(const int*)(smem + 26112) | *(const int*)(smem + 26624 + 26112) | *(const int*)(smem + 2 * 26624 + 26112) |
            *(const int*)(smem + 3 * 26624 + 26112);
    f = __builtin_amdgcn_readfirstlane(f);
    if (!f) break;
    asm volatile("s_waitcnt vmcnt(0) lgkmcnt(0)" ::: "memory");
    BARRIER();
#pragma unroll
    for (int qb = 0; qb < 3; ++qb) mrun[qb] += mtrue[qb];
    FOR8(J, issueK(0, 0, IC(J));)
    FOR8(J, issueK(1, 1, IC(J));)
    FOR8(J, issueV(0, 2, IC(J));)
  }

  STAMP(9);
  u32x4 ob[3][4];
  float fa0[3], fa1[3];
#pragma unroll
  for (int qb = 0; qb < 3; ++qb) {
    f32x2 ml = *(const f32x2*)(preg + 24576 + qb * 512 + lane * 8);
    float mn = fmaxf(mrun[qb], ml[0]);
    float a0 = __builtin_amdgcn_exp2f(mrun[qb] - mn), a1 = __builtin_amdgcn_exp2f(ml[0] - mn);
    float inv = 1.0f / (lsum[qb] * a0 + ml[1] * a1);
    fa0[qb] = a0 * inv; fa1[qb] = a1 * inv;
  }
  auto combine = [&](auto off_tag) {
    constexpr int OFF = decltype(off_tag)::value;
#pragma unroll
    for (int qb = 0; qb < 3; ++qb)
#pragma unroll
      for (int d = 0; d < 8; ++d) {
        f32x4 pv = *(const f32x4*)(preg + (qb * 8 + d) * 1024 + lane * 16);
        f32x4 r = o[qb][OFF + d] * fa0[qb] + pv * fa1[qb];
        ob[qb][d >> 1][2 * (d & 1)] = cvtpk(r[0], r[1]); ob[qb][d >> 1][2 * (d & 1) + 1] = cvtpk(r[2], r[3]);
      }
  };
  if (kh) combine(IC(8)); else combine(IC(0));
  asm volatile("s_waitcnt lgkmcnt(0)" ::: "memory");
  BARRIER();
#pragma unroll
  for (int qb = 0; qb < 3; ++qb)
#pragma unroll
    for (int dp = 0; dp < 4; ++dp)
      *(u32x4*)(smem + (unsigned)(rg * 48 + qb * 16 + i16) * 512u + 16u * ((unsigned)(kh * 16 + dp * 4 + g) ^ (unsigned)i16)) = ob[qb][dp];
#pragma unroll
  for (int j = 0; j < 7; ++j) issueWo(2, j);
#pragma unroll
  for (int j = 0; j < 7; ++j) issueWo(3, j);
  STAMP(10);

  {
    f32x4 pa[6][7];
#pragma unroll
    for (int mb = 0; mb < 6; ++mb)
#pragma unroll
      for (int nb = 0; nb < 7; ++nb) pa[mb][nb] = (f32x4){0.f, 0.f, 0.f, 0.f};
    asm volatile("" : PIN7A(pa[0]), PIN7A(pa[1]), PIN7A(pa[2]));
    asm volatile("" : PIN7A(pa[3]), PIN7A(pa[4]), PIN7A(pa[5]));
    unsigned wof[7];
    {
      const unsigned r = (unsigned)(4 * g + (i16 >> 2));
#pragma unroll
      for (int nb = 0; nb < 7; ++nb) {
        const unsigned seg = (unsigned)(wid * 7 + nb);
        wof[nb] = WOS + r * 896u + 32u * (seg ^ ((r >> 1) & 3u)) + 8u * (unsigned)(i16 & 3);
      }
    }
    bf16x8 of[6], ofn[6];
    bf16x4 wlo[7], whi[7], wplo[3], wphi[3];
    STAMP(11);
    WAIT_VM(14);
    BARRIER();
    STAMP(12);
    {
      const char* ab = smem + (unsigned)i16 * 512u + 16u * ((unsigned)g ^ (unsigned)i16);
#pragma unroll
      for (int mb = 0; mb < 6; ++mb) ofn[mb] = *(const bf16x8*)(ab + mb * 8192);
#pragma unroll
      for (int nb = 0; nb < 3; ++nb) { wplo[nb] = tr_read(smem + 2 * 28672 + wof[nb]); wphi[nb] = tr_read(smem + 2 * 28672 + wof[nb] + 16 * 896); }
    }
#pragma unroll 1
    for (int ks = 0; ks < 8; ++ks) {
      STAMPL(13);
      if (ks > 0) { WAIT_VM(7); BARRIER(); }
      STAMPL(12);
      const char* wb = smem + (unsigned)((ks + 2) & 3) * 28672u;
      const char* wn = smem + (unsigned)((ks + 3) & 3) * 28672u;
      const char* an = smem + (unsigned)i16 * 512u + 16u * ((unsigned)(4 * ((ks + 1) & 7) + g) ^ (unsigned)i16);
#pragma unroll
      for (int mb = 0; mb < 6; ++mb) of[mb] = ofn[mb];
#pragma unroll
      for (int nb = 0; nb < 3; ++nb) { wlo[nb] = wplo[nb]; whi[nb] = wphi[nb]; }
      SCHED();
#pragma unroll
      for (int nb = 0; nb < 7; ++nb) {
        bf16x8 wf = __builtin_shufflevector(wlo[nb], whi[nb], 0, 1, 2, 3, 4, 5, 6, 7);
#pragma unroll
        for (int mb = 0; mb < 6; ++mb) {
          if (mb == 1) {
            if (nb + 3 < 7) { wlo[nb + 3] = tr_read(wb + wof[nb + 3]); whi[nb + 3] = tr_read(wb + wof[nb + 3] + 16 * 896); }
            else { wplo[nb - 4] = tr_read(wn + wof[nb - 4]); wphi[nb - 4] = tr_read(wn + wof[nb - 4] + 16 * 896); }
          }
          if (mb == 3 && ks > 0) issueWo(ks + 3, nb);
          if (mb == 5 && nb < 6) ofn[nb] = *(const bf16x8*)(an + nb * 8192);
          mfma_acc(pa[mb][nb], wf, of[mb]);
          SCHED();
        }
      }
    }
    STAMP(13);
    asm volatile("s_nop 7\n\ts_nop 7" : PIN7A(pa[0]), PIN7A(pa[1]), PIN7A(pa[2]));
    asm volatile("" : PIN7A(pa[3]), PIN7A(pa[4]), PIN7A(pa[5]));
    u16* pp = Part + ((size_t)(h * 16 + b * 2 + rh) * 4 + wid) * (21 * 512) + lane * 8;
#pragma unroll
    for (int mp = 0; mp < 3; ++mp)
#pragma unroll
      for (int nb = 0; nb < 7; ++nb) {
        const f32x4 x0 = pa[2 * mp][nb], x1 = pa[2 * mp + 1][nb];
        u32x4 w = {cvtpk(x0[0], x0[1]), cvtpk(x0[2], x0[3]), cvtpk(x1[0], x1[1]), cvtpk(x1[2], x1[3])};
        *(u32x4*)(pp + (mp * 7 + nb) * 512) = w;
      }
  }
  STAMP(14);
  STAMP_END();
}

__global__ __launch_bounds__(256) void reduce_kernel(const u16* __restrict__ Part, u16* __restrict__ out) {
  const int gw = blockIdx.x * 4 + (threadIdx.x >> 6), lane = threadIdx.x & 63;
  const u16* p = Part + (size_t)gw * 512 + lane * 8;
  u32x4 v[16];
#pragma unroll
  for (int h = 0; h < 16; ++h) v[h] = __builtin_nontemporal_load((const u32x4*)(p + (size_t)h * (16 * 4 * 21 * 512)));
  float a[8] = {0.f, 0.f, 0.f, 0.f, 0.f, 0.f, 0.f, 0.f};
#pragma unroll
  for (int h = 0; h < 16; ++h)
#pragma unroll
    for (int e = 0; e < 4; ++e) {
      a[2 * e] += __uint_as_float(v[h][e] << 16);
      a[2 * e + 1] += __uint_as_float(v[h][e] & 0xffff0000u);
    }
  const int blk = gw % 21, rw = gw / 21, w = rw & 3, rt = rw >> 2, mp = blk / 7, nb = blk - mp * 7;
  u16* op = out + (size_t)(rt * 96 + mp * 32 + (lane & 15)) * DM + w * 112 + nb * 16 + (lane >> 4) * 4;
  *(u32x2*)op = (u32x2){cvtpk(a[0], a[1]), cvtpk(a[2], a[3])};
  *(u32x2*)(op + 16 * DM) = (u32x2){cvtpk(a[4], a[5]), cvtpk(a[6], a[7])};
  asm volatile(".Lall_code_end:");
}

extern "C" void kernel_launch(void* const* d_in, const int* in_sizes, int n_in, void* d_out, int out_size,
                              void* d_ws, size_t ws_size, hipStream_t stream) {
  const u16* x = (const u16*)d_in[0];
  const u16* kc = (const u16*)d_in[1];
  const u16* vc = (const u16*)d_in[2];
  const u16* wq = (const u16*)d_in[3];
  const u16* wo = (const u16*)d_in[4];
  u16* out = (u16*)d_out;
  u16* part = (u16*)d_ws;
  (void)hipFuncSetAttribute((const void*)attn_kernel, hipFuncAttributeMaxDynamicSharedMemorySize, ATT_LDS);
  attn_kernel<<<256, 256, ATT_LDS, stream>>>(x, wq, kc, vc, wo, part);
  reduce_kernel<<<336, 256, 0, stream>>>(part, out);
}
```

```cpp
#include <hip/hip_runtime.h>
#include <hip/hip_bf16.h>

typedef unsigned short u16;
typedef __attribute__((ext_vector_type(8))) short bf16x8;
typedef __attribute__((ext_vector_type(4))) short bf16x4;
typedef __attribute__((ext_vector_type(4))) float f32x4;
typedef __attribute__((ext_vector_type(2))) float f32x2;
typedef __attribute__((ext_vector_type(2))) __bf16 bf16x2_t;
typedef __attribute__((ext_vector_type(4))) unsigned u32x4;
typedef __attribute__((ext_vector_type(2))) unsigned u32x2;
typedef __attribute__((address_space(3))) bf16x4* lds_b64_ptr;

#define DI __device__ __forceinline__

constexpr int NB = 8, SQ = 192, DM = 448, NH = 16, SK = 1024, DH = 256;
constexpr int HD = NH * DH;

DI unsigned cvtpk(float lo, float hi) {
  f32x2 v = {lo, hi};
  bf16x2_t b = __builtin_convertvector(v, bf16x2_t);
  return __builtin_bit_cast(unsigned, b);
}
DI bf16x4 tr_read(const char* p) {
  return __builtin_amdgcn_ds_read_tr16_b64_v4i16((lds_b64_ptr)(p));
}

constexpr int SLOT = 32768;
constexpr int NSLOT = 5;
constexpr int ATT_LDS = SLOT * NSLOT;
constexpr float SCL = 0.0625f * 1.4426950408889634f;
constexpr float THR = 40.0f;

#define WAIT_VM(n) asm volatile("s_waitcnt vmcnt(" #n ") lgkmcnt(0)" ::: "memory")
#define WAIT_VMO(n) asm volatile("s_waitcnt vmcnt(" #n ")" ::: "memory")
#define BARRIER() __builtin_amdgcn_s_barrier()

#define SCHED() __builtin_amdgcn_sched_barrier(0)
#define STAMPL(k)
#define STAMP_END2()
#define STAMP(k)
#define STAMP_INIT()
#define STAMP_END()
DI void mfma_acc(f32x4& d, bf16x8 a, bf16x8 b) {
  asm volatile("v_mfma_f32_16x16x32_bf16 %0, %1, %2, %0" : "+a"(d) : "v"(a), "v"(b));
}
DI void mfma_v(f32x4& d, bf16x8 a, bf16x8 b) {
  asm volatile("v_mfma_f32_16x16x32_bf16 %0, %1, %2, %0" : "+v"(d) : "v"(a), "v"(b));
}
DI void mfma_v0(f32x4& d, bf16x8 a, bf16x8 b) {
  asm volatile("v_mfma_f32_16x16x32_bf16 %0, %1, %2, 0" : "=&v"(d) : "v"(a), "v"(b));
}

DI void mfma_acc0(f32x4& d, bf16x8 a, bf16x8 b) {
  asm volatile("v_mfma_f32_16x16x32_bf16 %0, %1, %2, 0" : "=a"(d) : "v"(a), "v"(b));
}
DI void mfma_vc(f32x4& d, bf16x8 a, bf16x8 b, f32x4 c) {
  asm volatile("v_mfma_f32_16x16x32_bf16 %0, %1, %2, %3" : "=&v"(d) : "v"(a), "v"(b), "v"(c));
}
#define PIN8A(x) "+a"((x)[0]), "+a"((x)[1]), "+a"((x)[2]), "+a"((x)[3]), "+a"((x)[4]), "+a"((x)[5]), "+a"((x)[6]), "+a"((x)[7])
#define PIN7A(x) "+a"((x)[0]), "+a"((x)[1]), "+a"((x)[2]), "+a"((x)[3]), "+a"((x)[4]), "+a"((x)[5]), "+a"((x)[6])
DI float max3f(float a, float b, float c) { float r; asm("v_max3_f32 %0, %1, %2, %3" : "=v"(r) : "v"(a), "v"(b), "v"(c)); return r; }
DI float fadd_s(float a, float b) { float r; asm("v_add_f32_e32 %0, %1, %2" : "=v"(r) : "v"(a), "v"(b)); return r; }

template <int OFF>
DI void dma_piece(unsigned ldsdst, unsigned voff, __amdgpu_buffer_rsrc_t rsrc, unsigned soff) {
  asm volatile("s_mov_b32 m0, %0\n\ts_nop 0\n\tbuffer_load_dwordx4 %1, %2, %3 offen offset:%4 lds"
               :: "s"(ldsdst), "v"(voff), "s"(rsrc), "s"(soff), "i"(OFF) : "memory");
}
DI void dma_piece_w(unsigned ldsdst, unsigned voff, __amdgpu_buffer_rsrc_t rsrc, unsigned soff) {
  asm volatile("s_mov_b32 m0, %0\n\ts_nop 0\n\tbuffer_load_dwordx4 %1, %2, %3 offen lds"
               :: "s"(ldsdst), "v"(voff), "s"(rsrc), "s"(soff) : "memory");
}
DI void dma_touch(unsigned ldsdst, unsigned voff, __amdgpu_buffer_rsrc_t rsrc) {
  asm volatile("s_mov_b32 m0, %0\n\ts_nop 0\n\tbuffer_load_dword %1, %2, 0 offen lds" :: "s"(ldsdst), "v"(voff), "s"(rsrc) : "memory");
}
DI float xmax_rows(float v) {
  auto a = __builtin_amdgcn_permlane16_swap(__float_as_uint(v), __float_as_uint(v), false, false);
  v = fmaxf(__uint_as_float(a[0]), __uint_as_float(a[1]));
  auto c = __builtin_amdgcn_permlane32_swap(__float_as_uint(v), __float_as_uint(v), false, false);
  return fmaxf(__uint_as_float(c[0]), __uint_as_float(c[1]));
}
DI float xsum_rows(float v) {
  auto a = __builtin_amdgcn_permlane16_swap(__float_as_uint(v), __float_as_uint(v), false, false);
  v = __uint_as_float(a[0]) + __uint_as_float(a[1]);
  auto c = __builtin_amdgcn_permlane32_swap(__float_as_uint(v), __float_as_uint(v), false, false);
  return __uint_as_float(c[0]) + __uint_as_float(c[1]);
}

__global__ __launch_bounds__(256, 1) void attn_kernel(const u16* __restrict__ X, const u16* __restrict__ Wq,
                                                      const u16* __restrict__ Kc, const u16* __restrict__ Vc,
                                                      const u16* __restrict__ Wo, u16* __restrict__ Part) {
  extern __shared__ __attribute__((aligned(16))) char smem[];
  STAMP_INIT();
  const int tid = threadIdx.x, lane = tid & 63;
  const int wid = __builtin_amdgcn_readfirstlane(tid >> 6);
  const int rg = wid & 1, kh = wid >> 1;
  const int bid = blockIdx.x;
  const int idx = bid >> 3, rh = idx & 1, h = 2 * (bid & 7) + ((idx >> 1) & 1), b = idx >> 2;
  const int g = lane >> 4, i16 = lane & 15;
  const unsigned ldsb = (unsigned)(size_t)smem;
  const int q0 = rh * 96 + rg * 48;
  const unsigned hoff = __builtin_amdgcn_readfirstlane((unsigned)h * 512u);
  const char* Kbase = (const char*)Kc + (size_t)(b * NH + h) * SK * DH * 2;
  const char* Vbase = (const char*)Vc + (size_t)(b * NH + h) * SK * DH * 2;
  __amdgpu_buffer_rsrc_t rK = __builtin_amdgcn_make_buffer_rsrc((void*)Kbase, (short)0, SK * DH * 2, 0x00020000);
  __amdgpu_buffer_rsrc_t rV = __builtin_amdgcn_make_buffer_rsrc((void*)Vbase, (short)0, SK * DH * 2, 0x00020000);
  __amdgpu_buffer_rsrc_t rW = __builtin_amdgcn_make_buffer_rsrc((void*)Wq, (short)0, DM * HD * 2, 0x00020000);
  __amdgpu_buffer_rsrc_t rWo = __builtin_amdgcn_make_buffer_rsrc((void*)(Wo + (size_t)h * DH * DM), (short)0, DH * DM * 2, 0x00020000);
  const unsigned wsoff = (unsigned)wid * 8192u;
  unsigned kdo[8], vdo[4], wdo[4];
  {
    const unsigned hi = lane >> 5, l32 = lane & 31;
#pragma unroll
    for (int j = 0; j < 8; ++j) kdo[j] = hi * 512u + 16u * (l32 ^ (2u * j + hi));
#pragma unroll
    for (int j = 0; j < 4; ++j) vdo[j] = hi * 512u + 16u * (l32 ^ ((2u * j + hi) << 1));
#pragma unroll
    for (int j = 0; j < 4; ++j) wdo[j] = hi * 8192u + 16u * (l32 ^ (((2u * (j & 1) + hi) | ((j >> 1) << 2)) << 1));
  }
  auto issueK = [&](int tl, int slot, auto jt) {
    constexpr int j = decltype(jt)::value;
    dma_piece<(j & 3) * 1024>(ldsb + (unsigned)slot * SLOT + wsoff + 4096u * (j >> 2), kdo[j], rK,
                              (unsigned)tl * 32768u + wsoff + 4096u * (j >> 2));
  };
  auto issueV = [&](int tl, int slot, auto jt) {
    constexpr int j = decltype(jt)::value;
    dma_piece<(j & 3) * 1024>(ldsb + (unsigned)slot * SLOT + wsoff + 4096u * (j >> 2), vdo[j & 3], rV,
                              (unsigned)tl * 32768u + wsoff + 4096u * (j >> 2));
  };
  auto issueW = [&](int c, int slot, int j) {
    dma_piece_w(ldsb + (unsigned)slot * SLOT + wsoff + 1024u * j, wdo[(j & 1) + 2 * (j >> 2)], rW,
                (unsigned)(64 * c + 16 * wid + 2 * j) * 8192u + hoff);
  };
#define FOR8(J, ...) { { constexpr int J = 0; __VA_ARGS__ } { constexpr int J = 1; __VA_ARGS__ } { constexpr int J = 2; __VA_ARGS__ } { constexpr int J = 3; __VA_ARGS__ } \
                       { constexpr int J = 4; __VA_ARGS__ } { constexpr int J = 5; __VA_ARGS__ } { constexpr int J = 6; __VA_ARGS__ } { constexpr int J = 7; __VA_ARGS__ } }
#define IC(x) std::integral_constant<int, (x)>{}

  {
    __amdgpu_buffer_rsrc_t rX = __builtin_amdgcn_make_buffer_rsrc((void*)(X + (size_t)(b * SQ + rh * 96) * DM), (short)0, 96 * DM * 2, 0x00020000);
#pragma unroll
    for (int j = 0; j < 21; ++j) {
      const unsigned n = (unsigned)(wid * 21 + j) * 64u + lane, r = n / 56u, c = n - r * 56u;
      dma_piece_w(ldsb + (unsigned)(wid * 21 + j) * 1024u, r * 896u + 16u * (c ^ ((r >> 1) & 7u)), rX, 0u);
    }
  }
#pragma unroll
  for (int c = 0; c < 2; ++c)
#pragma unroll
    for (int j = 0; j < 8; ++j) issueW(c, c + 3, j);
  {
    unsigned long long pc; unsigned csz;
    asm volatile("s_getpc_b64 %0\n.Lattn_code_begin:\n\ts_mov_b32 %1, .Lall_code_end-.Lattn_code_begin" : "=s"(pc), "=s"(csz));
    __amdgpu_buffer_rsrc_t rC = __builtin_amdgcn_make_buffer_rsrc((void*)pc, (short)0, (int)(csz <= 65536u ? csz : 0u), 0x00020000);
    const unsigned tdst = ldsb + 90112u + (unsigned)wid * 1024u;
    dma_touch(tdst, (unsigned)tid * 128u + (unsigned)(idx & 1) * 32768u, rC);
    const unsigned line = (unsigned)((b * 2 + rh) * 112 + (tid & 127));
    const bool on = (tid & 127) < 112;
    if (wid < 2) dma_touch(tdst + 256u, on ? line * 128u : 0x40000000u, rWo);
    else dma_touch(tdst + 256u, on ? (line >> 2) * 8192u + (line & 3u) * 128u + hoff : 0x40000000u, rW);
  }

  unsigned koff[4], voff[8];
#pragma unroll
  for (int a = 0; a < 4; ++a) koff[a] = (unsigned)(kh * 32 + i16) * 512u + 16u * (unsigned)((4 * a + g) ^ i16);
  {
    const int row = 4 * g + (i16 >> 2), r7 = row & 7;
#pragma unroll
    for (int bb = 0; bb < 8; ++bb) voff[bb] = (unsigned)(kh * 32 + row) * 512u + 32u * (unsigned)(bb ^ r7) + 8u * (unsigned)(i16 & 3);
  }

  bf16x8 qf[3][8];
  {
    f32x4 qa[3][8];
    unsigned woff[8];
    {
      const int q4 = i16 >> 2, f = q4 | ((g & 1) << 2);
#pragma unroll
      for (int db = 0; db < 8; ++db) woff[db] = (unsigned)(8 * g + q4) * 512u + (unsigned)kh * 256u + 32u * (unsigned)(db ^ f) + 8u * (unsigned)(i16 & 3);
    }
    bf16x8 xf[3][14];
    STAMP(0);
    WAIT_VM(16);
    BARRIER();
    STAMP(1);
#pragma unroll
    for (int qb = 0; qb < 3; ++qb) {
      const unsigned r = (unsigned)(rg * 48 + qb * 16 + i16);
#pragma unroll
      for (int kk = 0; kk < 14; ++kk)
        xf[qb][kk] = *(const bf16x8*)(smem + r * 896u + 128u * (kk >> 1) + 16u * ((unsigned)(4 * (kk & 1) + g) ^ ((r >> 1) & 7u)));
    }
    bf16x4 wplo[4], wphi[4];
    auto qstep = [&](auto ctag) {
      constexpr int c = decltype(ctag)::value;
      STAMPL(2);
      if (c == 0) WAIT_VM(0); else WAIT_VMO(16);
      BARRIER();
      STAMPL(3);
      const char* wb0 = smem + ((c + 3) % 5) * SLOT;
      const char* wn0 = smem + ((c + 4) % 5) * SLOT;
      constexpr int PF = 4;
      bf16x4 wlo[16], whi[16];
      auto wread = [&](int i) {
        const char* p = wb0 + woff[i & 7] + (i >> 3) * 16384;
        wlo[i] = tr_read(p); whi[i] = tr_read(p + 2048);
      };
#pragma unroll
      for (int i = 0; i < PF; ++i) { if (c == 0) wread(i); else { wlo[i] = wplo[i]; whi[i] = wphi[i]; } }
      SCHED();
#pragma unroll
      for (int i = 0; i < 16; ++i) {
        bf16x8 wf = __builtin_shufflevector(wlo[i], whi[i], 0, 1, 2, 3, 4, 5, 6, 7);
#pragma unroll
        for (int qb = 0; qb < 3; ++qb) {
          if (qb == 0) {
            if (i + PF < 16) wread(i + PF);
            else if (c < 6) { wplo[i + PF - 16] = tr_read(wn0 + woff[i + PF - 16]); wphi[i + PF - 16] = tr_read(wn0 + woff[i + PF - 16] + 2048); }
          }
          if (c == 0) {
            if (qb == 1) { if (i < 8) issueW(2, 0, i); else issueW(4, 2, i - 8); }
            if (qb == 2 && i < 8) issueW(3, 1, i);
          } else if (qb == 2 && (i & 1)) {
            if (c < 3) issueW(c + 4, (c + 7) % 5, i >> 1);
            FOR8(J, if (J == (i >> 1)) { if (c == 3) issueK(0, 0, IC(J)); if (c == 4) issueK(1, 1, IC(J)); if (c == 5) issueV(0, 2, IC(J)); })
          }
          if (c == 0 && i < 8) mfma_acc0(qa[qb][i & 7], wf, xf[qb][0]);
          else mfma_acc(qa[qb][i & 7], wf, xf[qb][2 * c + (i >> 3)]);
          SCHED();
        }
      }
    };
    qstep(IC(0));
    qstep(IC(1)); qstep(IC(2)); qstep(IC(3)); qstep(IC(4)); qstep(IC(5)); qstep(IC(6));
    STAMP(2);
    asm volatile("s_nop 7\n\ts_nop 7" : PIN8A(qa[0]), PIN8A(qa[1]), PIN8A(qa[2]));
    asm volatile("s_waitcnt lgkmcnt(0)" ::: "memory");
    BARRIER();
#pragma unroll
    for (int qb = 0; qb < 3; ++qb)
#pragma unroll
      for (int db = 0; db < 8; ++db) {
        u32x2 w = {cvtpk(qa[qb][db][0] * SCL, qa[qb][db][1] * SCL), cvtpk(qa[qb][db][2] * SCL, qa[qb][db][3] * SCL)};
        const unsigned ch = 2u * (8u * kh + db) + (g >> 1);
        *(u32x2*)(smem + 3 * SLOT + (unsigned)(rg * 48 + qb * 16 + i16) * 512u + 16u * (ch ^ (unsigned)i16) + 8u * (g & 1)) = w;
      }
    asm volatile("s_waitcnt lgkmcnt(0)" ::: "memory");
    BARRIER();
#pragma unroll
    for (int qb = 0; qb < 3; ++qb)
#pragma unroll
      for (int ks = 0; ks < 8; ++ks)
        qf[qb][ks] = *(const bf16x8*)(smem + 3 * SLOT + (unsigned)(rg * 48 + qb * 16 + i16) * 512u + 16u * ((unsigned)(4 * ks + g) ^ (unsigned)i16));
  }

  f32x4 o[3][16];
  float mrun[3] = {0.f, 0.f, 0.f}, lsum[3], mtrue[3];
  f32x4 negm[3];
  int attempt = 0;
  int sl_c = 0, sl_i = 0;
  auto nxt = [](int x) { return x == NSLOT - 1 ? 0 : x + 1; };
  f32x4 sA[3][2], sB[3][2];
  bf16x8 pf[3];
#define ATT_PF 4
  constexpr int PF = ATT_PF;
  bf16x8 kpre[PF];
  bf16x4 vplo[PF], vphi[PF];

  constexpr unsigned WOS = 49152u;
  unsigned wodo[7];
  auto issueWo = [&](int ks, int j) {
    dma_piece_w(ldsb + WOS + (unsigned)((ks + 2) & 3) * 28672u + (unsigned)(wid * 7 + j) * 1024u, wodo[j], rWo, (unsigned)ks * 28672u);
  };

  auto phaseA = [&](f32x4 (&sn)[3][2], f32x4 (&sp)[3][2], int tk, auto mode_tag) {
    constexpr bool FIRST = decltype(mode_tag)::value == 1;
    constexpr bool ISSUE = decltype(mode_tag)::value == 0;
    if constexpr (FIRST) STAMP(4); else STAMPL(8);
    if constexpr (FIRST) WAIT_VM(8); else WAIT_VMO(16);
    BARRIER();
    STAMPL(5);
    const char* kb0 = smem + (unsigned)sl_c * SLOT;
    const char* nb0 = smem + (unsigned)nxt(sl_c) * SLOT;
    bf16x8 kf[16];
    unsigned pw[12];
    auto kread = [&](int i) { kf[i] = *(const bf16x8*)(kb0 + koff[(i >> 1) & 3] + (i >> 3) * 256 + (i & 1) * 8192); };
    if constexpr (FIRST) {
#pragma unroll
      for (int i = 0; i < PF; ++i) kread(i);
    } else {
#pragma unroll
      for (int i = 0; i < PF; ++i) kf[i] = kpre[i];
    }
    SCHED();
#pragma unroll
    for (int i = 0; i < 16; ++i) {
      const int ks = i >> 1, kb = i & 1;
#pragma unroll
      for (int qb = 0; qb < 3; ++qb) {
        const int m = 3 * i + qb, u = m % 6, blk = m / 6;
        if (qb == 0) {
          if (i + PF < 16) kread(i + PF);
          else if constexpr (FIRST) kpre[i + PF - 16] = *(const bf16x8*)(nb0 + koff[((i + PF - 16) >> 1) & 3] + ((i + PF - 16) & 1) * 8192);
          else { const char* p = nb0 + voff[i + PF - 16]; vplo[i + PF - 16] = tr_read(p); vphi[i + PF - 16] = tr_read(p + 8192); }
          if constexpr (!FIRST) {
            const int done = 3 * blk + (u == 3 ? 2 : 0), prev = (u == 3) ? 3 * blk : (blk > 0 ? 3 * (blk - 1) + 2 : 0);
#pragma unroll
            for (int k = 0; k < 12; ++k)
              if (2 * k + 2 <= done && 2 * k + 2 > prev) pw[k] = cvtpk(sp[k >> 2][(k >> 1) & 1][2 * (k & 1)], sp[k >> 2][(k >> 1) & 1][2 * (k & 1) + 1]);
          }
        }
        if (u == 5) {
          if constexpr (ISSUE) { FOR8(J, if (J == blk) issueK(tk, sl_i, IC(J));) }
        }
        if constexpr (FIRST) { if (qb == 2) { FOR8(J, if (J == i) issueK(2, 3, IC(J)); if (J + 8 == i) issueV(1, 4, IC(J));) } }
        if constexpr (!FIRST) {
          if (u == 1 || u == 2 || u == 4) {
            const int e = 3 * blk + (u == 1 ? 0 : u == 2 ? 1 : 2), eq = e >> 3, ej = e & 7;
            sp[eq][ej >> 2][ej & 3] = __builtin_amdgcn_exp2f(sp[eq][ej >> 2][ej & 3]);
          }
        }
        if (ks == 0) { if constexpr (FIRST) mfma_v0(sn[qb][kb], kf[i], qf[qb][ks]); else mfma_vc(sn[qb][kb], kf[i], qf[qb][ks], negm[qb]); }
        else mfma_v(sn[qb][kb], kf[i], qf[qb][ks]);
        SCHED();
      }
    }
    if constexpr (!FIRST) {
      pw[11] = cvtpk(sp[2][1][2], sp[2][1][3]);
#pragma unroll
      for (int qb = 0; qb < 3; ++qb) {
        u32x4 w = {pw[4 * qb], pw[4 * qb + 1], pw[4 * qb + 2], pw[4 * qb + 3]};
        pf[qb] = __builtin_bit_cast(bf16x8, w);
      }
      sl_i = nxt(sl_i);
    }
    sl_c = nxt(sl_c);
  };
  auto softmax_only = [&](f32x4 (&sp)[3][2]) {
#pragma unroll
    for (int qb = 0; qb < 3; ++qb) {
#pragma unroll
      for (int e = 0; e < 8; ++e) sp[qb][e >> 2][e & 3] = __builtin_amdgcn_exp2f(sp[qb][e >> 2][e & 3]);
      u32x4 w = {cvtpk(sp[qb][0][0], sp[qb][0][1]), cvtpk(sp[qb][0][2], sp[qb][0][3]), cvtpk(sp[qb][1][0], sp[qb][1][1]), cvtpk(sp[qb][1][2], sp[qb][1][3])};
      pf[qb] = __builtin_bit_cast(bf16x8, w);
    }
  };
  auto phaseB = [&](f32x4 (&sn)[3][2], f32x4 (&sp)[3][2], int tv, auto mode_tag) {
    constexpr int MODE = decltype(mode_tag)::value;
    STAMPL(6);
    if constexpr (MODE == 2) WAIT_VM(14); else WAIT_VMO(16);
    BARRIER();
    STAMPL(7);
    const char* vb0 = smem + (unsigned)sl_c * SLOT;
    const char* nb0 = smem + (unsigned)nxt(sl_c) * SLOT;
    bf16x4 vlo[16], vhi[16];
    auto vread = [&](int db) {
      const char* p = vb0 + voff[db & 7] + (db >> 3) * 256;
      vlo[db] = tr_read(p); vhi[db] = tr_read(p + 8192);
    };
    if constexpr (MODE == 2) {
#pragma unroll
      for (int i = 0; i < PF; ++i) vread(i);
    } else {
#pragma unroll
      for (int i = 0; i < PF; ++i) { vlo[i] = vplo[i]; vhi[i] = vphi[i]; }
    }
    SCHED();
#pragma unroll
    for (int db = 0; db < 16; ++db) {
      bf16x8 vf = __builtin_shufflevector(vlo[db], vhi[db], 0, 1, 2, 3, 4, 5, 6, 7);
#pragma unroll
      for (int qb = 0; qb < 3; ++qb) {
        const int m = 3 * db + qb, u = m % 6, blk = m / 6;
        if (qb == 0) {
          if (db + PF < 16) vread(db + PF);
          else if constexpr (MODE == 0) kpre[db + PF - 16] = *(const bf16x8*)(nb0 + koff[((db + PF - 16) >> 1) & 3] + ((db + PF - 16) & 1) * 8192);
        }
        if constexpr (MODE == 0) { if (u == 5) { FOR8(J, if (J == blk) issueV(tv, sl_i, IC(J));) } }
        if constexpr (MODE == 1) { if (qb == 2 && db < 14) issueWo(db / 7, db % 7); }
        if (u == 1 || u == 2) {
#pragma unroll
          for (int z = 0; z < (u == 1 ? 2 : 1); ++z) {
            const int e = 3 * blk + (u == 1 ? z : 2), eq = e >> 3, ej = e & 7;
            lsum[eq] = fadd_s(lsum[eq], sp[eq][ej >> 2][ej & 3]);
          }
        }
        if constexpr (MODE < 2) {
          if (u == 2 || (u == 4 && (blk & 1) == 0)) {
            const int x = (3 * blk) / 2 + (u == 4 ? 1 : ((blk & 1) ? 1 : 0)) , eq = x >> 2, ej = x & 3;
            mtrue[eq] = max3f(mtrue[eq], sn[eq][ej >> 1][(ej & 1) * 2], sn[eq][ej >> 1][(ej & 1) * 2 + 1]);
          }
        }
        mfma_acc(o[qb][db], vf, pf[qb]);
        SCHED();
      }
    }
    sl_c = nxt(sl_c); sl_i = nxt(sl_i);
  };
  using T_ = std::true_type;
  using F_ = std::false_type;
  char* myreg = smem + wid * 26624;
  char* preg = smem + (wid ^ 2) * 26624;

  for (;; ++attempt) {
    sl_c = 0; sl_i = 0;
#pragma unroll
    for (int qb = 0; qb < 3; ++qb) {
      lsum[qb] = 0.f;
#pragma unroll
      for (int db = 0; db < 16; ++db) o[qb][db] = (f32x4){0.f, 0.f, 0.f, 0.f};
    }
    asm volatile("" : PIN8A(o[0]), PIN8A(o[1]), PIN8A(o[2]));
    asm volatile("" : PIN8A(o[0] + 8), PIN8A(o[1] + 8), PIN8A(o[2] + 8));
    phaseA(sA, sB, 0, IC(1));
    asm volatile("s_nop 7" ::: "memory");
    SCHED();
#pragma unroll
    for (int qb = 0; qb < 3; ++qb) {
      asm volatile("" : "+v"(sA[qb][0]), "+v"(sA[qb][1]));
      float r = max3f(max3f(sA[qb][0][0], sA[qb][0][1], sA[qb][0][2]), max3f(sA[qb][0][3], sA[qb][1][0], sA[qb][1][1]),
                      fmaxf(sA[qb][1][2], sA[qb][1][3]));
      if (attempt == 0) mrun[qb] = xmax_rows(r);
      mtrue[qb] = r - mrun[qb];
      negm[qb] = (f32x4){-mrun[qb], -mrun[qb], -mrun[qb], -mrun[qb]};
      sA[qb][0] += negm[qb]; sA[qb][1] += negm[qb];
    }
#pragma unroll 1
    for (int t = 0; t < 14; t += 2) {
      phaseA(sB, sA, t + 3, IC(0));
      phaseB(sB, sA, t + 2, IC(0));
      phaseA(sA, sB, t + 4, IC(0));
      phaseB(sA, sB, t + 3, IC(0));
    }
    phaseA(sB, sA, 17, IC(2));
    {
      unsigned lz = lane;
      asm volatile("" : "+v"(lz));
#pragma unroll
      for (int j = 0; j < 7; ++j) {
        const unsigned n = (unsigned)(wid * 7 + j) * 64u + lz, r = n / 56u, c = n - r * 56u;
        wodo[j] = r * 896u + 16u * (c ^ (((r >> 1) & 3u) << 1));
      }
    }
    phaseB(sB, sA, 16, IC(1));
    softmax_only(sB);
    sl_c = nxt(sl_c);
    phaseB(sA, sB, 17, IC(2));

    STAMP(8);
    bool bad = false;
#pragma unroll
    for (int qb = 0; qb < 3; ++qb) {
      lsum[qb] = xsum_rows(lsum[qb]);
      mtrue[qb] = xmax_rows(mtrue[qb]);
      bad |= !(mtrue[qb] <= THR);
    }
    const int wbad = (attempt == 0 && __any(bad)) ? 1 : 0;
    asm volatile("s_nop 7\n\ts_nop 7" : PIN8A(o[0]), PIN8A(o[1]), PIN8A(o[2]));
    asm volatile("" : PIN8A(o[0] + 8), PIN8A(o[1] + 8), PIN8A(o[2] + 8));
    asm volatile("s_waitcnt lgkmcnt(0)" ::: "memory");
    BARRIER();
    if (kh) {
#pragma unroll
      for (int qb = 0; qb < 3; ++qb)
#pragma unroll
        for (int d = 0; d < 8; ++d) *(f32x4*)(myreg + (qb * 8 + d) * 1024 + lane * 16) = o[qb][d];
    } else {
#pragma unroll
      for (int qb = 0; qb < 3; ++qb)
#pragma unroll
        for (int d = 0; d < 8; ++d) *(f32x4*)(myreg + (qb * 8 + d) * 1024 + lane * 16) = o[qb][8 + d];
    }
#pragma unroll
    for (int qb = 0; qb < 3; ++qb) *(f32x2*)(myreg + 24576 + qb * 512 + lane * 8) = (f32x2){mrun[qb], lsum[qb]};
    if (lane == 0) *(int*)(myreg + 26112) = wbad;
    asm volatile("s_waitcnt lgkmcnt(0)" ::: "memory");
    BARRIER();
    int f = *(const int*)(smem + 26112) | *(const int*)(smem + 26624 + 26112) | *(const int*)(smem + 2 * 26624 + 26112) |
            *(const int*)(smem + 3 * 26624 + 26112);
    f = __builtin_amdgcn_readfirstlane(f);
    if (!f) break;
    asm volatile("s_waitcnt vmcnt(0) lgkmcnt(0)" ::: "memory");
    BARRIER();
#pragma unroll
    for (int qb = 0; qb < 3; ++qb) mrun[qb] += mtrue[qb];
    FOR8(J, issueK(0, 0, IC(J));)
    FOR8(J, issueK(1, 1, IC(J));)
    FOR8(J, issueV(0, 2, IC(J));)
  }

  STAMP(9);
  u32x4 ob[3][4];
  float fa0[3], fa1[3];
#pragma unroll
  for (int qb = 0; qb < 3; ++qb) {
    f32x2 ml = *(const f32x2*)(preg + 24576 + qb * 512 + lane * 8);
    float mn = fmaxf(mrun[qb], ml[0]);
    float a0 = __builtin_amdgcn_exp2f(mrun[qb] - mn), a1 = __builtin_amdgcn_exp2f(ml[0] - mn);
    float inv = 1.0f / (lsum[qb] * a0 + ml[1] * a1);
    fa0[qb] = a0 * inv; fa1[qb] = a1 * inv;
  }
  auto combine = [&](auto off_tag) {
    constexpr int OFF = decltype(off_tag)::value;
#pragma unroll
    for (int qb = 0; qb < 3; ++qb)
#pragma unroll
      for (int d = 0; d < 8; ++d) {
        f32x4 pv = *(const f32x4*)(preg + (qb * 8 + d) * 1024 + lane * 16);
        f32x4 r = o[qb][OFF + d] * fa0[qb] + pv * fa1[qb];
        ob[qb][d >> 1][2 * (d & 1)] = cvtpk(r[0], r[1]); ob[qb][d >> 1][2 * (d & 1) + 1] = cvtpk(r[2], r[3]);
      }
  };
  if (kh) combine(IC(8)); else combine(IC(0));
  asm volatile("s_waitcnt lgkmcnt(0)" ::: "memory");
  BARRIER();
#pragma unroll
  for (int qb = 0; qb < 3; ++qb)
#pragma unroll
    for (int dp = 0; dp < 4; ++dp)
      *(u32x4*)(smem + (unsigned)(rg * 48 + qb * 16 + i16) * 512u + 16u * ((unsigned)(kh * 16 + dp * 4 + g) ^ (unsigned)i16)) = ob[qb][dp];
#pragma unroll
  for (int j = 0; j < 7; ++j) issueWo(2, j);
#pragma unroll
  for (int j = 0; j < 7; ++j) issueWo(3, j);
  STAMP(10);

  {
    f32x4 pa[6][7];
#pragma unroll
    for (int mb = 0; mb < 6; ++mb)
#pragma unroll
      for (int nb = 0; nb < 7; ++nb) pa[mb][nb] = (f32x4){0.f, 0.f, 0.f, 0.f};
    asm volatile("" : PIN7A(pa[0]), PIN7A(pa[1]), PIN7A(pa[2]));
    asm volatile("" : PIN7A(pa[3]), PIN7A(pa[4]), PIN7A(pa[5]));
    unsigned wof[7];
    {
      const unsigned r = (unsigned)(4 * g + (i16 >> 2));
#pragma unroll
      for (int nb = 0; nb < 7; ++nb) {
        const unsigned seg = (unsigned)(wid * 7 + nb);
        wof[nb] = WOS + r * 896u + 32u * (seg ^ ((r >> 1) & 3u)) + 8u * (unsigned)(i16 & 3);
      }
    }
    bf16x8 of[6], ofn[6];
    bf16x4 wlo[7], whi[7], wplo[3], wphi[3];
    STAMP(11);
    WAIT_VM(14);
    BARRIER();
    STAMP(12);
    {
      const char* ab = smem + (unsigned)i16 * 512u + 16u * ((unsigned)g ^ (unsigned)i16);
#pragma unroll
      for (int mb = 0; mb < 6; ++mb) ofn[mb] = *(const bf16x8*)(ab + mb * 8192);
#pragma unroll
      for (int nb = 0; nb < 3; ++nb) { wplo[nb] = tr_read(smem + 2 * 28672 + wof[nb]); wphi[nb] = tr_read(smem + 2 * 28672 + wof[nb] + 16 * 896); }
    }
#pragma unroll 1
    for (int ks = 0; ks < 8; ++ks) {
      STAMPL(13);
      if (ks > 0) { WAIT_VMO(7); BARRIER(); }
      STAMPL(12);
      const char* wb = smem + (unsigned)((ks + 2) & 3) * 28672u;
      const char* wn = smem + (unsigned)((ks + 3) & 3) * 28672u;
      const char* an = smem + (unsigned)i16 * 512u + 16u * ((unsigned)(4 * ((ks + 1) & 7) + g) ^ (unsigned)i16);
#pragma unroll
      for (int mb = 0; mb < 6; ++mb) of[mb] = ofn[mb];
#pragma unroll
      for (int nb = 0; nb < 3; ++nb) { wlo[nb] = wplo[nb]; whi[nb] = wphi[nb]; }
      SCHED();
#pragma unroll
      for (int nb = 0; nb < 7; ++nb) {
        bf16x8 wf = __builtin_shufflevector(wlo[nb], whi[nb], 0, 1, 2, 3, 4, 5, 6, 7);
#pragma unroll
        for (int mb = 0; mb < 6; ++mb) {
          if (mb == 1) {
            if (nb + 3 < 7) { wlo[nb + 3] = tr_read(wb + wof[nb + 3]); whi[nb + 3] = tr_read(wb + wof[nb + 3] + 16 * 896); }
            else { wplo[nb - 4] = tr_read(wn + wof[nb - 4]); wphi[nb - 4] = tr_read(wn + wof[nb - 4] + 16 * 896); }
          }
          if (mb == 3 && ks > 0) issueWo(ks + 3, nb);
          if (mb == 5 && nb < 6) ofn[nb] = *(const bf16x8*)(an + nb * 8192);
          mfma_acc(pa[mb][nb], wf, of[mb]);
          SCHED();
        }
      }
    }
    STAMP(13);
    asm volatile("s_nop 7\n\ts_nop 7" : PIN7A(pa[0]), PIN7A(pa[1]), PIN7A(pa[2]));
    asm volatile("" : PIN7A(pa[3]), PIN7A(pa[4]), PIN7A(pa[5]));
    u16* pp = Part + ((size_t)(h * 16 + b * 2 + rh) * 4 + wid) * (21 * 512) + lane * 8;
#pragma unroll
    for (int mp = 0; mp < 3; ++mp)
#pragma unroll
      for (int nb = 0; nb < 7; ++nb) {
        const f32x4 x0 = pa[2 * mp][nb], x1 = pa[2 * mp + 1][nb];
        u32x4 w = {cvtpk(x0[0], x0[1]), cvtpk(x0[2], x0[3]), cvtpk(x1[0], x1[1]), cvtpk(x1[2], x1[3])};
        __builtin_nontemporal_store(w, (u32x4*)(pp + (mp * 7 + nb) * 512));
      }
  }
  STAMP(14);
  STAMP_END();
}

__global__ __launch_bounds__(256) void reduce_kernel(const u16* __restrict__ Part, u16* __restrict__ out) {
  __shared__ __attribute__((aligned(16))) float red[2][64][8];
  const int wv = threadIdx.x >> 6, lane = threadIdx.x & 63, hh = wv >> 1;
  const int gw = blockIdx.x * 2 + (wv & 1);
  const u16* p = Part + (size_t)gw * 512 + lane * 8 + (size_t)hh * 8 * (16 * 4 * 21 * 512);
  u32x4 v[8];
#pragma unroll
  for (int h = 0; h < 8; ++h) v[h] = __builtin_nontemporal_load((const u32x4*)(p + (size_t)h * (16 * 4 * 21 * 512)));
  float a[8] = {0.f, 0.f, 0.f, 0.f, 0.f, 0.f, 0.f, 0.f};
#pragma unroll
  for (int h = 0; h < 8; ++h)
#pragma unroll
    for (int e = 0; e < 4; ++e) {
      a[2 * e] += __uint_as_float(v[h][e] << 16);
      a[2 * e + 1] += __uint_as_float(v[h][e] & 0xffff0000u);
    }
  if (hh) {
    *(f32x4*)&red[wv & 1][lane][0] = (f32x4){a[0], a[1], a[2], a[3]};
    *(f32x4*)&red[wv & 1][lane][4] = (f32x4){a[4], a[5], a[6], a[7]};
  }
  __syncthreads();
  if (!hh) {
    const f32x4 r0 = *(const f32x4*)&red[wv & 1][lane][0], r1 = *(const f32x4*)&red[wv & 1][lane][4];
    const int blk = gw % 21, rw = gw / 21, w = rw & 3, rt = rw >> 2, mp = blk / 7, nb = blk - mp * 7;
    u16* op = out + (size_t)(rt * 96 + mp * 32 + (lane & 15)) * DM + w * 112 + nb * 16 + (lane >> 4) * 4;
    *(u32x2*)op = (u32x2){cvtpk(a[0] + r0[0], a[1] + r0[1]), cvtpk(a[2] + r0[2], a[3] + r0[3])};
    *(u32x2*)(op + 16 * DM) = (u32x2){cvtpk(a[4] + r1[0], a[5] + r1[1]), cvtpk(a[6] + r1[2], a[7] + r1[3])};
  }
  asm volatile(".Lall_code_end:");
}

extern "C" void kernel_launch(void* const* d_in, const int* in_sizes, int n_in, void* d_out, int out_size,
                              void* d_ws, size_t ws_size, hipStream_t stream) {
  const u16* x = (const u16*)d_in[0];
  const u16* kc = (const u16*)d_in[1];
  const u16* vc = (const u16*)d_in[2];
  const u16* wq = (const u16*)d_in[3];
  const u16* wo = (const u16*)d_in[4];
  u16* out = (u16*)d_out;
  u16* part = (u16*)d_ws;
  (void)hipFuncSetAttribute((const void*)attn_kernel, hipFuncAttributeMaxDynamicSharedMemorySize, ATT_LDS);
  attn_kernel<<<256, 256, ATT_LDS, stream>>>(x, wq, kc, vc, wo, part);
  reduce_kernel<<<672, 256, 0, stream>>>(part, out);
}
```

```cpp
#include <hip/hip_runtime.h>
#include <hip/hip_bf16.h>

typedef unsigned short u16;
typedef __attribute__((ext_vector_type(8))) short bf16x8;
typedef __attribute__((ext_vector_type(4))) short bf16x4;
typedef __attribute__((ext_vector_type(4))) float f32x4;
typedef __attribute__((ext_vector_type(2))) float f32x2;
typedef __attribute__((ext_vector_type(2))) __bf16 bf16x2_t;
typedef __attribute__((ext_vector_type(4))) unsigned u32x4;
typedef __attribute__((ext_vector_type(2))) unsigned u32x2;
typedef __attribute__((address_space(3))) bf16x4* lds_b64_ptr;

#define DI __device__ __forceinline__

constexpr int NB = 8, SQ = 192, DM = 448, NH = 16, SK = 1024, DH = 256;
constexpr int HD = NH * DH;

DI unsigned cvtpk(float lo, float hi) {
  f32x2 v = {lo, hi};
  bf16x2_t b = __builtin_convertvector(v, bf16x2_t);
  return __builtin_bit_cast(unsigned, b);
}
DI bf16x4 tr_read(const char* p) {
  return __builtin_amdgcn_ds_read_tr16_b64_v4i16((lds_b64_ptr)(p));
}

constexpr int SLOT = 32768;
constexpr int NSLOT = 5;
constexpr int ATT_LDS = SLOT * NSLOT;
constexpr float SCL = 0.0625f * 1.4426950408889634f;
#define ATT_THR 40.0f
constexpr float THR = ATT_THR;

#define WAIT_VM(n) asm volatile("s_waitcnt vmcnt(" #n ") lgkmcnt(0)" ::: "memory")
#define WAIT_VMO(n) asm volatile("s_waitcnt vmcnt(" #n ")" ::: "memory")
#define BARRIER() __builtin_amdgcn_s_barrier()

#define SCHED() __builtin_amdgcn_sched_barrier(0)
#define STAMPL(k)
#define STAMP_END2()
#define STAMP(k)
#define STAMP_INIT()
#define STAMP_END()
DI void mfma_acc(f32x4& d, bf16x8 a, bf16x8 b) {
  asm volatile("v_mfma_f32_16x16x32_bf16 %0, %1, %2, %0" : "+a"(d) : "v"(a), "v"(b));
}
DI void mfma_v(f32x4& d, bf16x8 a, bf16x8 b) {
  asm volatile("v_mfma_f32_16x16x32_bf16 %0, %1, %2, %0" : "+v"(d) : "v"(a), "v"(b));
}
DI void mfma_v0(f32x4& d, bf16x8 a, bf16x8 b) {
  asm volatile("v_mfma_f32_16x16x32_bf16 %0, %1, %2, 0" : "=&v"(d) : "v"(a), "v"(b));
}

DI void mfma_acc0(f32x4& d, bf16x8 a, bf16x8 b) {
  asm volatile("v_mfma_f32_16x16x32_bf16 %0, %1, %2, 0" : "=a"(d) : "v"(a), "v"(b));
}
DI void mfma_vc(f32x4& d, bf16x8 a, bf16x8 b, f32x4 c) {
  asm volatile("v_mfma_f32_16x16x32_bf16 %0, %1, %2, %3" : "=&v"(d) : "v"(a), "v"(b), "v"(c));
}
#define PIN8A(x) "+a"((x)[0]), "+a"((x)[1]), "+a"((x)[2]), "+a"((x)[3]), "+a"((x)[4]), "+a"((x)[5]), "+a"((x)[6]), "+a"((x)[7])
#define PIN7A(x) "+a"((x)[0]), "+a"((x)[1]), "+a"((x)[2]), "+a"((x)[3]), "+a"((x)[4]), "+a"((x)[5]), "+a"((x)[6])
DI float max3f(float a, float b, float c) { float r; asm("v_max3_f32 %0, %1, %2, %3" : "=v"(r) : "v"(a), "v"(b), "v"(c)); return r; }
DI float fadd_s(float a, float b) { float r; asm("v_add_f32_e32 %0, %1, %2" : "=v"(r) : "v"(a), "v"(b)); return r; }

template <int OFF>
DI void dma_piece(unsigned ldsdst, unsigned voff, __amdgpu_buffer_rsrc_t rsrc, unsigned soff) {
  asm volatile("s_mov_b32 m0, %0\n\ts_nop 0\n\tbuffer_load_dwordx4 %1, %2, %3 offen offset:%4 nt lds"
               :: "s"(ldsdst), "v"(voff), "s"(rsrc), "s"(soff), "i"(OFF) : "memory");
}
DI void dma_piece_w(unsigned ldsdst, unsigned voff, __amdgpu_buffer_rsrc_t rsrc, unsigned soff) {
  asm volatile("s_mov_b32 m0, %0\n\ts_nop 0\n\tbuffer_load_dwordx4 %1, %2, %3 offen lds"
               :: "s"(ldsdst), "v"(voff), "s"(rsrc), "s"(soff) : "memory");
}
DI void dma_touch(unsigned ldsdst, unsigned voff, __amdgpu_buffer_rsrc_t rsrc) {
  asm volatile("s_mov_b32 m0, %0\n\ts_nop 0\n\tbuffer_load_dword %1, %2, 0 offen lds" :: "s"(ldsdst), "v"(voff), "s"(rsrc) : "memory");
}
DI float xmax_rows(float v) {
  auto a = __builtin_amdgcn_permlane16_swap(__float_as_uint(v), __float_as_uint(v), false, false);
  v = fmaxf(__uint_as_float(a[0]), __uint_as_float(a[1]));
  auto c = __builtin_amdgcn_permlane32_swap(__float_as_uint(v), __float_as_uint(v), false, false);
  return fmaxf(__uint_as_float(c[0]), __uint_as_float(c[1]));
}
DI float xsum_rows(float v) {
  auto a = __builtin_amdgcn_permlane16_swap(__float_as_uint(v), __float_as_uint(v), false, false);
  v = __uint_as_float(a[0]) + __uint_as_float(a[1]);
  auto c = __builtin_amdgcn_permlane32_swap(__float_as_uint(v), __float_as_uint(v), false, false);
  return __uint_as_float(c[0]) + __uint_as_float(c[1]);
}

__global__ __launch_bounds__(256, 1) void attn_kernel(const u16* __restrict__ X, const u16* __restrict__ Wq,
                                                      const u16* __restrict__ Kc, const u16* __restrict__ Vc,
                                                      const u16* __restrict__ Wo, u16* __restrict__ Part) {
  extern __shared__ __attribute__((aligned(16))) char smem[];
  STAMP_INIT();
  const int tid = threadIdx.x, lane = tid & 63;
  const int wid = __builtin_amdgcn_readfirstlane(tid >> 6);
  const int rg = wid & 1, kh = wid >> 1;
  const int bid = blockIdx.x;
  const int idx = bid >> 3, rh = idx & 1, h = 2 * (bid & 7) + ((idx >> 1) & 1), b = idx >> 2;
  const int g = lane >> 4, i16 = lane & 15;
  const unsigned ldsb = (unsigned)(size_t)smem;
  const int q0 = rh * 96 + rg * 48;
  const unsigned hoff = __builtin_amdgcn_readfirstlane((unsigned)h * 512u);
  const char* Kbase = (const char*)Kc + (size_t)(b * NH + h) * SK * DH * 2;
  const char* Vbase = (const char*)Vc + (size_t)(b * NH + h) * SK * DH * 2;
  __amdgpu_buffer_rsrc_t rK = __builtin_amdgcn_make_buffer_rsrc((void*)Kbase, (short)0, SK * DH * 2, 0x00020000);
  __amdgpu_buffer_rsrc_t rV = __builtin_amdgcn_make_buffer_rsrc((void*)Vbase, (short)0, SK * DH * 2, 0x00020000);
  __amdgpu_buffer_rsrc_t rW = __builtin_amdgcn_make_buffer_rsrc((void*)Wq, (short)0, DM * HD * 2, 0x00020000);
  __amdgpu_buffer_rsrc_t rWo = __builtin_amdgcn_make_buffer_rsrc((void*)(Wo + (size_t)h * DH * DM), (short)0, DH * DM * 2, 0x00020000);
  const unsigned wsoff = (unsigned)wid * 8192u;
  unsigned kdo[8], vdo[4], wdo[4];
  {
    const unsigned hi = lane >> 5, l32 = lane & 31;
#pragma unroll
    for (int j = 0; j < 8; ++j) kdo[j] = hi * 512u + 16u * (l32 ^ (2u * j + hi));
#pragma unroll
    for (int j = 0; j < 4; ++j) vdo[j] = hi * 512u + 16u * (l32 ^ ((2u * j + hi) << 1));
#pragma unroll
    for (int j = 0; j < 4; ++j) wdo[j] = hi * 8192u + 16u * (l32 ^ (((2u * (j & 1) + hi) | ((j >> 1) << 2)) << 1));
  }
  auto issueK = [&](int tl, int slot, auto jt) {
    constexpr int j = decltype(jt)::value;
    dma_piece<(j & 3) * 1024>(ldsb + (unsigned)slot * SLOT + wsoff + 4096u * (j >> 2), kdo[j], rK,
                              (unsigned)tl * 32768u + wsoff + 4096u * (j >> 2));
  };
  auto issueV = [&](int tl, int slot, auto jt) {
    constexpr int j = decltype(jt)::value;
    dma_piece<(j & 3) * 1024>(ldsb + (unsigned)slot * SLOT + wsoff + 4096u * (j >> 2), vdo[j & 3], rV,
                              (unsigned)tl * 32768u + wsoff + 4096u * (j >> 2));
  };
  auto issueW = [&](int c, int slot, int j) {
    dma_piece_w(ldsb + (unsigned)slot * SLOT + wsoff + 1024u * j, wdo[(j & 1) + 2 * (j >> 2)], rW,
                (unsigned)(64 * c + 16 * wid + 2 * j) * 8192u + hoff);
  };
#define FOR8(J, ...) { { constexpr int J = 0; __VA_ARGS__ } { constexpr int J = 1; __VA_ARGS__ } { constexpr int J = 2; __VA_ARGS__ } { constexpr int J = 3; __VA_ARGS__ } \
                       { constexpr int J = 4; __VA_ARGS__ } { constexpr int J = 5; __VA_ARGS__ } { constexpr int J = 6; __VA_ARGS__ } { constexpr int J = 7; __VA_ARGS__ } }
#define IC(x) std::integral_constant<int, (x)>{}

  {
    __amdgpu_buffer_rsrc_t rX = __builtin_amdgcn_make_buffer_rsrc((void*)(X + (size_t)(b * SQ + rh * 96) * DM), (short)0, 96 * DM * 2, 0x00020000);
#pragma unroll
    for (int j = 0; j < 21; ++j) {
      const unsigned n = (unsigned)(wid * 21 + j) * 64u + lane, r = n / 56u, c = n - r * 56u;
      dma_piece_w(ldsb + (unsigned)(wid * 21 + j) * 1024u, r * 896u + 16u * (c ^ ((r >> 1) & 7u)), rX, 0u);
    }
  }
#pragma unroll
  for (int c = 0; c < 2; ++c)
#pragma unroll
    for (int j = 0; j < 8; ++j) issueW(c, c + 3, j);
  {
    unsigned long long pc; unsigned csz;
    asm volatile("s_getpc_b64 %0\n.Lattn_code_begin:\n\ts_mov_b32 %1, .Lall_code_end-.Lattn_code_begin" : "=s"(pc), "=s"(csz));
    __amdgpu_buffer_rsrc_t rC = __builtin_amdgcn_make_buffer_rsrc((void*)pc, (short)0, (int)(csz <= 65536u ? csz : 0u), 0x00020000);
    const unsigned tdst = ldsb + 90112u + (unsigned)wid * 1024u;
    dma_touch(tdst, (unsigned)tid * 128u + (unsigned)(idx & 1) * 32768u, rC);
    const unsigned line = (unsigned)((b * 2 + rh) * 112 + (tid & 127));
    const bool on = (tid & 127) < 112;
    if (wid < 2) dma_touch(tdst + 256u, on ? line * 128u : 0x40000000u, rWo);
    else dma_touch(tdst + 256u, on ? (line >> 2) * 8192u + (line & 3u) * 128u + hoff : 0x40000000u, rW);
  }

  unsigned koff[4], voff[8];
#pragma unroll
  for (int a = 0; a < 4; ++a) koff[a] = (unsigned)(kh * 32 + i16) * 512u + 16u * (unsigned)((4 * a + g) ^ i16);
  {
    const int row = 4 * g + (i16 >> 2), r7 = row & 7;
#pragma unroll
    for (int bb = 0; bb < 8; ++bb) voff[bb] = (unsigned)(kh * 32 + row) * 512u + (unsigned)kh * 256u + 32u * (unsigned)(bb ^ r7) + 8u * (unsigned)(i16 & 3);
  }

  const int xoff = 256 - 512 * kh;

  bf16x8 qf[3][8];
  {
    f32x4 qa[3][8];
    unsigned woff[8];
    {
      const int q4 = i16 >> 2, f = q4 | ((g & 1) << 2);
#pragma unroll
      for (int db = 0; db < 8; ++db) woff[db] = (unsigned)(8 * g + q4) * 512u + (unsigned)kh * 256u + 32u * (unsigned)(db ^ f) + 8u * (unsigned)(i16 & 3);
    }
    bf16x8 xf[3][14];
    STAMP(0);
    WAIT_VM(16);
    BARRIER();
    STAMP(1);
#pragma unroll
    for (int qb = 0; qb < 3; ++qb) {
      const unsigned r = (unsigned)(rg * 48 + qb * 16 + i16);
#pragma unroll
      for (int kk = 0; kk < 14; ++kk)
        xf[qb][kk] = *(const bf16x8*)(smem + r * 896u + 128u * (kk >> 1) + 16u * ((unsigned)(4 * (kk & 1) + g) ^ ((r >> 1) & 7u)));
    }
    bf16x4 wplo[4], wphi[4];
    auto qstep = [&](auto ctag) {
      constexpr int c = decltype(ctag)::value;
      STAMPL(2);
      if (c == 0) WAIT_VM(0); else WAIT_VMO(16);
      BARRIER();
      STAMPL(3);
      const char* wb0 = smem + ((c + 3) % 5) * SLOT;
      const char* wn0 = smem + ((c + 4) % 5) * SLOT;
      constexpr int PF = 4;
      bf16x4 wlo[16], whi[16];
      auto wread = [&](int i) {
        const char* p = wb0 + woff[i & 7] + (i >> 3) * 16384;
        wlo[i] = tr_read(p); whi[i] = tr_read(p + 2048);
      };
#pragma unroll
      for (int i = 0; i < PF; ++i) { if (c == 0) wread(i); else { wlo[i] = wplo[i]; whi[i] = wphi[i]; } }
      SCHED();
#pragma unroll
      for (int i = 0; i < 16; ++i) {
        bf16x8 wf = __builtin_shufflevector(wlo[i], whi[i], 0, 1, 2, 3, 4, 5, 6, 7);
#pragma unroll
        for (int qb = 0; qb < 3; ++qb) {
          if (qb == 0) {
            if (i + PF < 16) wread(i + PF);
            else if (c < 6) { wplo[i + PF - 16] = tr_read(wn0 + woff[i + PF - 16]); wphi[i + PF - 16] = tr_read(wn0 + woff[i + PF - 16] + 2048); }
          }
          if (c == 0) {
            if (qb == 1) { if (i < 8) issueW(2, 0, i); else issueW(4, 2, i - 8); }
            if (qb == 2 && i < 8) issueW(3, 1, i);
          } else if (qb == 2 && (i & 1)) {
            if (c < 3) issueW(c + 4, (c + 7) % 5, i >> 1);
            FOR8(J, if (J == (i >> 1)) { if (c == 3) issueK(0, 0, IC(J)); if (c == 4) issueK(1, 1, IC(J)); if (c == 5) issueV(0, 2, IC(J)); })
          }
          if (c == 0 && i < 8) mfma_acc0(qa[qb][i & 7], wf, xf[qb][0]);
          else mfma_acc(qa[qb][i & 7], wf, xf[qb][2 * c + (i >> 3)]);
          SCHED();
        }
      }
    };
    qstep(IC(0));
    qstep(IC(1)); qstep(IC(2)); qstep(IC(3)); qstep(IC(4)); qstep(IC(5)); qstep(IC(6));
    STAMP(2);
    asm volatile("s_nop 7\n\ts_nop 7" : PIN8A(qa[0]), PIN8A(qa[1]), PIN8A(qa[2]));
    asm volatile("s_waitcnt lgkmcnt(0)" ::: "memory");
    BARRIER();
#pragma unroll
    for (int qb = 0; qb < 3; ++qb)
#pragma unroll
      for (int db = 0; db < 8; ++db) {
        u32x2 w = {cvtpk(qa[qb][db][0] * SCL, qa[qb][db][1] * SCL), cvtpk(qa[qb][db][2] * SCL, qa[qb][db][3] * SCL)};
        const unsigned ch = 2u * (8u * kh + db) + (g >> 1);
        *(u32x2*)(smem + 3 * SLOT + (unsigned)(rg * 48 + qb * 16 + i16) * 512u + 16u * (ch ^ (unsigned)i16) + 8u * (g & 1)) = w;
      }
    asm volatile("s_waitcnt lgkmcnt(0)" ::: "memory");
    BARRIER();
#pragma unroll
    for (int qb = 0; qb < 3; ++qb)
#pragma unroll
      for (int ks = 0; ks < 8; ++ks)
        qf[qb][ks] = *(const bf16x8*)(smem + 3 * SLOT + (unsigned)(rg * 48 + qb * 16 + i16) * 512u + 16u * ((unsigned)(4 * ks + g) ^ (unsigned)i16));
  }

  f32x4 o[3][16];
  float mrun[3] = {0.f, 0.f, 0.f}, lsum[3], mtrue[3];
  f32x4 negm[3];
  int attempt = 0;
  int sl_c = 0, sl_i = 0;
  auto nxt = [](int x) { return x == NSLOT - 1 ? 0 : x + 1; };
  f32x4 sA[3][2], sB[3][2];
  bf16x8 pf[3];
#define ATT_PF 4
  constexpr int PF = ATT_PF;
  bf16x8 kpre[PF];
  bf16x4 vplo[PF], vphi[PF];

  constexpr unsigned WOS = 49152u;
  unsigned wodo[7];
  auto issueWo = [&](int ks, int j) {
    dma_piece_w(ldsb + WOS + (unsigned)((ks + 2) & 3) * 28672u + (unsigned)(wid * 7 + j) * 1024u, wodo[j], rWo, (unsigned)ks * 28672u);
  };

  auto phaseA = [&](f32x4 (&sn)[3][2], f32x4 (&sp)[3][2], int tk, auto mode_tag) {
    constexpr bool FIRST = decltype(mode_tag)::value == 1;
    constexpr bool ISSUE = decltype(mode_tag)::value == 0;
    if constexpr (FIRST) STAMP(4); else STAMPL(8);
    if constexpr (FIRST) WAIT_VM(8); else WAIT_VMO(16);
    BARRIER();
    STAMPL(5);
    const char* kb0 = smem + (unsigned)sl_c * SLOT;
    const char* nb0 = smem + (unsigned)nxt(sl_c) * SLOT;
    bf16x8 kf[16];
    unsigned pw[12];
    auto kread = [&](int i) { kf[i] = *(const bf16x8*)(kb0 + koff[(i >> 1) & 3] + (i >> 3) * 256 + (i & 1) * 8192); };
    if constexpr (FIRST) {
#pragma unroll
      for (int i = 0; i < PF; ++i) kread(i);
    } else {
#pragma unroll
      for (int i = 0; i < PF; ++i) kf[i] = kpre[i];
    }
    SCHED();
#pragma unroll
    for (int i = 0; i < 16; ++i) {
      const int ks = i >> 1, kb = i & 1;
#pragma unroll
      for (int qb = 0; qb < 3; ++qb) {
        const int m = 3 * i + qb, u = m % 6, blk = m / 6;
        if (qb == 0) {
          if (i + PF < 16) kread(i + PF);
          else if constexpr (FIRST) kpre[i + PF - 16] = *(const bf16x8*)(nb0 + koff[((i + PF - 16) >> 1) & 3] + ((i + PF - 16) & 1) * 8192);
          else { const char* p = nb0 + voff[i + PF - 16]; vplo[i + PF - 16] = tr_read(p); vphi[i + PF - 16] = tr_read(p + 8192); }
          if constexpr (!FIRST) {
            const int done = 3 * blk + (u == 3 ? 2 : 0), prev = (u == 3) ? 3 * blk : (blk > 0 ? 3 * (blk - 1) + 2 : 0);
#pragma unroll
            for (int k = 0; k < 12; ++k)
              if (2 * k + 2 <= done && 2 * k + 2 > prev) pw[k] = cvtpk(sp[k >> 2][(k >> 1) & 1][2 * (k & 1)], sp[k >> 2][(k >> 1) & 1][2 * (k & 1) + 1]);
          }
        }
        if (u == 5) {
          if constexpr (ISSUE) { FOR8(J, if (J == blk) issueK(tk, sl_i, IC(J));) }
        }
        if constexpr (FIRST) { if (qb == 2) { FOR8(J, if (J == i) issueK(2, 3, IC(J)); if (J + 8 == i) issueV(1, 4, IC(J));) } }
        if constexpr (!FIRST) {
          if (u == 1 || u == 2 || u == 4) {
            const int e = 3 * blk + (u == 1 ? 0 : u == 2 ? 1 : 2), eq = e >> 3, ej = e & 7;
            sp[eq][ej >> 2][ej & 3] = __builtin_amdgcn_exp2f(sp[eq][ej >> 2][ej & 3]);
          }
        }
        if (ks == 0) { if constexpr (FIRST) mfma_v0(sn[qb][kb], kf[i], qf[qb][ks]); else mfma_vc(sn[qb][kb], kf[i], qf[qb][ks], negm[qb]); }
        else mfma_v(sn[qb][kb], kf[i], qf[qb][ks]);
        SCHED();
      }
    }
    if constexpr (!FIRST) {
      pw[11] = cvtpk(sp[2][1][2], sp[2][1][3]);
#pragma unroll
      for (int qb = 0; qb < 3; ++qb) {
        u32x4 w = {pw[4 * qb], pw[4 * qb + 1], pw[4 * qb + 2], pw[4 * qb + 3]};
        pf[qb] = __builtin_bit_cast(bf16x8, w);
      }
      sl_i = nxt(sl_i);
    }
    sl_c = nxt(sl_c);
  };
  auto softmax_only = [&](f32x4 (&sp)[3][2]) {
#pragma unroll
    for (int qb = 0; qb < 3; ++qb) {
#pragma unroll
      for (int e = 0; e < 8; ++e) sp[qb][e >> 2][e & 3] = __builtin_amdgcn_exp2f(sp[qb][e >> 2][e & 3]);
      u32x4 w = {cvtpk(sp[qb][0][0], sp[qb][0][1]), cvtpk(sp[qb][0][2], sp[qb][0][3]), cvtpk(sp[qb][1][0], sp[qb][1][1]), cvtpk(sp[qb][1][2], sp[qb][1][3])};
      pf[qb] = __builtin_bit_cast(bf16x8, w);
    }
  };
  auto phaseB = [&](f32x4 (&sn)[3][2], f32x4 (&sp)[3][2], int tv, auto mode_tag) {
    constexpr int MODE = decltype(mode_tag)::value;
    STAMPL(6);
    if constexpr (MODE == 2) WAIT_VM(14); else WAIT_VMO(16);
    BARRIER();
    STAMPL(7);
    const char* vb0 = smem + (unsigned)sl_c * SLOT;
    const char* nb0 = smem + (unsigned)nxt(sl_c) * SLOT;
    bf16x4 vlo[16], vhi[16];
    auto vread = [&](int db) {
      const char* p = vb0 + voff[db & 7] + (db >> 3) * xoff;
      vlo[db] = tr_read(p); vhi[db] = tr_read(p + 8192);
    };
    if constexpr (MODE == 2) {
#pragma unroll
      for (int i = 0; i < PF; ++i) vread(i);
    } else {
#pragma unroll
      for (int i = 0; i < PF; ++i) { vlo[i] = vplo[i]; vhi[i] = vphi[i]; }
    }
    SCHED();
#pragma unroll
    for (int db = 0; db < 16; ++db) {
      bf16x8 vf = __builtin_shufflevector(vlo[db], vhi[db], 0, 1, 2, 3, 4, 5, 6, 7);
#pragma unroll
      for (int qb = 0; qb < 3; ++qb) {
        const int m = 3 * db + qb, u = m % 6, blk = m / 6;
        if (qb == 0) {
          if (db + PF < 16) vread(db + PF);
          else if constexpr (MODE == 0) kpre[db + PF - 16] = *(const bf16x8*)(nb0 + koff[((db + PF - 16) >> 1) & 3] + ((db + PF - 16) & 1) * 8192);
        }
        if constexpr (MODE == 0) { if (u == 5) { FOR8(J, if (J == blk) issueV(tv, sl_i, IC(J));) } }
        if constexpr (MODE == 1) { if (qb == 2 && db < 14) issueWo(db / 7, db % 7); }
        if (u == 1 || u == 2) {
#pragma unroll
          for (int z = 0; z < (u == 1 ? 2 : 1); ++z) {
            const int e = 3 * blk + (u == 1 ? z : 2), eq = e >> 3, ej = e & 7;
            lsum[eq] = fadd_s(lsum[eq], sp[eq][ej >> 2][ej & 3]);
          }
        }
        if constexpr (MODE < 2) {
          if (u == 2 || (u == 4 && (blk & 1) == 0)) {
            const int x = (3 * blk) / 2 + (u == 4 ? 1 : ((blk & 1) ? 1 : 0)) , eq = x >> 2, ej = x & 3;
            mtrue[eq] = max3f(mtrue[eq], sn[eq][ej >> 1][(ej & 1) * 2], sn[eq][ej >> 1][(ej & 1) * 2 + 1]);
          }
        }
        mfma_acc(o[qb][db], vf, pf[qb]);
        SCHED();
      }
    }
    sl_c = nxt(sl_c); sl_i = nxt(sl_i);
  };
  using T_ = std::true_type;
  using F_ = std::false_type;
  char* myreg = smem + wid * 26624;
  char* preg = smem + (wid ^ 2) * 26624;

  for (;; ++attempt) {
    sl_c = 0; sl_i = 0;
#pragma unroll
    for (int qb = 0; qb < 3; ++qb) {
      lsum[qb] = 0.f;
#pragma unroll
      for (int db = 0; db < 16; ++db) o[qb][db] = (f32x4){0.f, 0.f, 0.f, 0.f};
    }
    asm volatile("" : PIN8A(o[0]), PIN8A(o[1]), PIN8A(o[2]));
    asm volatile("" : PIN8A(o[0] + 8), PIN8A(o[1] + 8), PIN8A(o[2] + 8));
    phaseA(sA, sB, 0, IC(1));
    asm volatile("s_nop 7" ::: "memory");
    SCHED();
#pragma unroll
    for (int qb = 0; qb < 3; ++qb) {
      asm volatile("" : "+v"(sA[qb][0]), "+v"(sA[qb][1]));
      float r = max3f(max3f(sA[qb][0][0], sA[qb][0][1], sA[qb][0][2]), max3f(sA[qb][0][3], sA[qb][1][0], sA[qb][1][1]),
                      fmaxf(sA[qb][1][2], sA[qb][1][3]));
      if (attempt == 0) mrun[qb] = xmax_rows(r);
      mtrue[qb] = r - mrun[qb];
      negm[qb] = (f32x4){-mrun[qb], -mrun[qb], -mrun[qb], -mrun[qb]};
      sA[qb][0] += negm[qb]; sA[qb][1] += negm[qb];
    }
#pragma unroll 1
    for (int t = 0; t < 14; t += 2) {
      phaseA(sB, sA, t + 3, IC(0));
      phaseB(sB, sA, t + 2, IC(0));
      phaseA(sA, sB, t + 4, IC(0));
      phaseB(sA, sB, t + 3, IC(0));
    }
    phaseA(sB, sA, 17, IC(2));
    {
      unsigned lz = lane;
      asm volatile("" : "+v"(lz));
#pragma unroll
      for (int j = 0; j < 7; ++j) {
        const unsigned n = (unsigned)(wid * 7 + j) * 64u + lz, r = n / 56u, c = n - r * 56u;
        wodo[j] = r * 896u + 16u * (c ^ (((r >> 1) & 3u) << 1));
      }
    }
    phaseB(sB, sA, 16, IC(1));
    softmax_only(sB);
    sl_c = nxt(sl_c);
    phaseB(sA, sB, 17, IC(2));

    STAMP(8);
    bool bad = false;
#pragma unroll
    for (int qb = 0; qb < 3; ++qb) {
      lsum[qb] = xsum_rows(lsum[qb]);
      mtrue[qb] = xmax_rows(mtrue[qb]);
      bad |= !(mtrue[qb] <= THR);
    }
    const int wbad = (attempt == 0 && __any(bad)) ? 1 : 0;
    asm volatile("s_nop 7\n\ts_nop 7" : PIN8A(o[0]), PIN8A(o[1]), PIN8A(o[2]));
    asm volatile("" : PIN8A(o[0] + 8), PIN8A(o[1] + 8), PIN8A(o[2] + 8));
    asm volatile("s_waitcnt lgkmcnt(0)" ::: "memory");
    BARRIER();
#pragma unroll
    for (int qb = 0; qb < 3; ++qb)
#pragma unroll
      for (int d = 0; d < 8; ++d) *(f32x4*)(myreg + (qb * 8 + d) * 1024 + lane * 16) = o[qb][8 + d];
#pragma unroll
    for (int qb = 0; qb < 3; ++qb) *(f32x2*)(myreg + 24576 + qb * 512 + lane * 8) = (f32x2){mrun[qb], lsum[qb]};
    if (lane == 0) *(int*)(myreg + 26112) = wbad;
    asm volatile("s_waitcnt lgkmcnt(0)" ::: "memory");
    BARRIER();
    int f = *(const int*)(smem + 26112) | *(const int*)(smem + 26624 + 26112) | *(const int*)(smem + 2 * 26624 + 26112) |
            *(const int*)(smem + 3 * 26624 + 26112);
    f = __builtin_amdgcn_readfirstlane(f);
    if (!f) break;
    asm volatile("s_waitcnt vmcnt(0) lgkmcnt(0)" ::: "memory");
    BARRIER();
#pragma unroll
    for (int qb = 0; qb < 3; ++qb) mrun[qb] += mtrue[qb];
    FOR8(J, issueK(0, 0, IC(J));)
    FOR8(J, issueK(1, 1, IC(J));)
    FOR8(J, issueV(0, 2, IC(J));)
  }

  STAMP(9);
  u32x4 ob[3][4];
  float fa0[3], fa1[3];
#pragma unroll
  for (int qb = 0; qb < 3; ++qb) {
    f32x2 ml = *(const f32x2*)(preg + 24576 + qb * 512 + lane * 8);
    float mn = fmaxf(mrun[qb], ml[0]);
    float a0 = __builtin_amdgcn_exp2f(mrun[qb] - mn), a1 = __builtin_amdgcn_exp2f(ml[0] - mn);
    float inv = 1.0f / (lsum[qb] * a0 + ml[1] * a1);
    fa0[qb] = a0 * inv; fa1[qb] = a1 * inv;
  }
  auto combine = [&](auto off_tag) {
    constexpr int OFF = decltype(off_tag)::value;
#pragma unroll
    for (int qb = 0; qb < 3; ++qb)
#pragma unroll
      for (int d = 0; d < 8; ++d) {
        f32x4 pv = *(const f32x4*)(preg + (qb * 8 + d) * 1024 + lane * 16);
        f32x4 r = o[qb][OFF + d] * fa0[qb] + pv * fa1[qb];
        ob[qb][d >> 1][2 * (d & 1)] = cvtpk(r[0], r[1]); ob[qb][d >> 1][2 * (d & 1) + 1] = cvtpk(r[2], r[3]);
      }
  };
  combine(IC(0));
  asm volatile("s_waitcnt lgkmcnt(0)" ::: "memory");
  BARRIER();
#pragma unroll
  for (int qb = 0; qb < 3; ++qb)
#pragma unroll
    for (int dp = 0; dp < 4; ++dp)
      *(u32x4*)(smem + (unsigned)(rg * 48 + qb * 16 + i16) * 512u + 16u * ((unsigned)(kh * 16 + dp * 4 + g) ^ (unsigned)i16)) = ob[qb][dp];
#pragma unroll
  for (int j = 0; j < 7; ++j) issueWo(2, j);
#pragma unroll
  for (int j = 0; j < 7; ++j) issueWo(3, j);
  STAMP(10);

  {
    f32x4 pa[6][7];
#pragma unroll
    for (int mb = 0; mb < 6; ++mb)
#pragma unroll
      for (int nb = 0; nb < 7; ++nb) pa[mb][nb] = (f32x4){0.f, 0.f, 0.f, 0.f};
    asm volatile("" : PIN7A(pa[0]), PIN7A(pa[1]), PIN7A(pa[2]));
    asm volatile("" : PIN7A(pa[3]), PIN7A(pa[4]), PIN7A(pa[5]));
    unsigned wof[7];
    {
      const unsigned r = (unsigned)(4 * g + (i16 >> 2));
#pragma unroll
      for (int nb = 0; nb < 7; ++nb) {
        const unsigned seg = (unsigned)(wid * 7 + nb);
        wof[nb] = WOS + r * 896u + 32u * (seg ^ ((r >> 1) & 3u)) + 8u * (unsigned)(i16 & 3);
      }
    }
    bf16x8 of[6], ofn[6];
    bf16x4 wlo[7], whi[7], wplo[3], wphi[3];
    STAMP(11);
    WAIT_VM(14);
    BARRIER();
    STAMP(12);
    {
      const char* ab = smem + (unsigned)i16 * 512u + 16u * ((unsigned)g ^ (unsigned)i16);
#pragma unroll
      for (int mb = 0; mb < 6; ++mb) ofn[mb] = *(const bf16x8*)(ab + mb * 8192);
#pragma unroll
      for (int nb = 0; nb < 3; ++nb) { wplo[nb] = tr_read(smem + 2 * 28672 + wof[nb]); wphi[nb] = tr_read(smem + 2 * 28672 + wof[nb] + 16 * 896); }
    }
#pragma unroll 1
    for (int ks = 0; ks < 8; ++ks) {
      STAMPL(13);
      if (ks > 0) { WAIT_VMO(7); BARRIER(); }
      STAMPL(12);
      const char* wb = smem + (unsigned)((ks + 2) & 3) * 28672u;
      const char* wn = smem + (unsigned)((ks + 3) & 3) * 28672u;
      const char* an = smem + (unsigned)i16 * 512u + 16u * ((unsigned)(4 * ((ks + 1) & 7) + g) ^ (unsigned)i16);
#pragma unroll
      for (int mb = 0; mb < 6; ++mb) of[mb] = ofn[mb];
#pragma unroll
      for (int nb = 0; nb < 3; ++nb) { wlo[nb] = wplo[nb]; whi[nb] = wphi[nb]; }
      SCHED();
#pragma unroll
      for (int nb = 0; nb < 7; ++nb) {
        bf16x8 wf = __builtin_shufflevector(wlo[nb], whi[nb], 0, 1, 2, 3, 4, 5, 6, 7);
#pragma unroll
        for (int mb = 0; mb < 6; ++mb) {
          if (mb == 1) {
            if (nb + 3 < 7) { wlo[nb + 3] = tr_read(wb + wof[nb + 3]); whi[nb + 3] = tr_read(wb + wof[nb + 3] + 16 * 896); }
            else { wplo[nb - 4] = tr_read(wn + wof[nb - 4]); wphi[nb - 4] = tr_read(wn + wof[nb - 4] + 16 * 896); }
          }
          if (mb == 3 && ks > 0) issueWo(ks + 3, nb);
          if (mb == 5 && nb < 6) ofn[nb] = *(const bf16x8*)(an + nb * 8192);
          mfma_acc(pa[mb][nb], wf, of[mb]);
          SCHED();
        }
      }
    }
    STAMP(13);
    asm volatile("s_nop 7\n\ts_nop 7" : PIN7A(pa[0]), PIN7A(pa[1]), PIN7A(pa[2]));
    asm volatile("" : PIN7A(pa[3]), PIN7A(pa[4]), PIN7A(pa[5]));
    u16* pp = Part + ((size_t)(h * 16 + b * 2 + rh) * 4 + wid) * (21 * 512) + lane * 8;
#pragma unroll
    for (int mp = 0; mp < 3; ++mp)
#pragma unroll
      for (int nb = 0; nb < 7; ++nb) {
        const f32x4 x0 = pa[2 * mp][nb], x1 = pa[2 * mp + 1][nb];
        u32x4 w = {cvtpk(x0[0], x0[1]), cvtpk(x0[2], x0[3]), cvtpk(x1[0], x1[1]), cvtpk(x1[2], x1[3])};
        __builtin_nontemporal_store(w, (u32x4*)(pp + (mp * 7 + nb) * 512));
      }
  }
  STAMP(14);
  STAMP_END();
}

__global__ __launch_bounds__(256) void reduce_kernel(const u16* __restrict__ Part, u16* __restrict__ out) {
  __shared__ __attribute__((aligned(16))) float red[2][64][8];
  const int wv = threadIdx.x >> 6, lane = threadIdx.x & 63, hh = wv >> 1;
  const int gw = blockIdx.x * 2 + (wv & 1);
  const u16* p = Part + (size_t)gw * 512 + lane * 8 + (size_t)hh * 8 * (16 * 4 * 21 * 512);
  u32x4 v[8];
#pragma unroll
  for (int h = 0; h < 8; ++h) v[h] = __builtin_nontemporal_load((const u32x4*)(p + (size_t)h * (16 * 4 * 21 * 512)));
  float a[8] = {0.f, 0.f, 0.f, 0.f, 0.f, 0.f, 0.f, 0.f};
#pragma unroll
  for (int h = 0; h < 8; ++h)
#pragma unroll
    for (int e = 0; e < 4; ++e) {
      a[2 * e] += __uint_as_float(v[h][e] << 16);
      a[2 * e + 1] += __uint_as_float(v[h][e] & 0xffff0000u);
    }
  if (hh) {
    *(f32x4*)&red[wv & 1][lane][0] = (f32x4){a[0], a[1], a[2], a[3]};
    *(f32x4*)&red[wv & 1][lane][4] = (f32x4){a[4], a[5], a[6], a[7]};
  }
  __syncthreads();
  if (!hh) {
    const f32x4 r0 = *(const f32x4*)&red[wv & 1][lane][0], r1 = *(const f32x4*)&red[wv & 1][lane][4];
    const int blk = gw % 21, rw = gw / 21, w = rw & 3, rt = rw >> 2, mp = blk / 7, nb = blk - mp * 7;
    u16* op = out + (size_t)(rt * 96 + mp * 32 + (lane & 15)) * DM + w * 112 + nb * 16 + (lane >> 4) * 4;
    *(u32x2*)op = (u32x2){cvtpk(a[0] + r0[0], a[1] + r0[1]), cvtpk(a[2] + r0[2], a[3] + r0[3])};
    *(u32x2*)(op + 16 * DM) = (u32x2){cvtpk(a[4] + r1[0], a[5] + r1[1]), cvtpk(a[6] + r1[2], a[7] + r1[3])};
  }
  asm volatile(".Lall_code_end:");
}

extern "C" void kernel_launch(void* const* d_in, const int* in_sizes, int n_in, void* d_out, int out_size,
                              void* d_ws, size_t ws_size, hipStream_t stream) {
  const u16* x = (const u16*)d_in[0];
  const u16* kc = (const u16*)d_in[1];
  const u16* vc = (const u16*)d_in[2];
  const u16* wq = (const u16*)d_in[3];
  const u16* wo = (const u16*)d_in[4];
  u16* out = (u16*)d_out;
  u16* part = (u16*)d_ws;
  (void)hipFuncSetAttribute((const void*)attn_kernel, hipFuncAttributeMaxDynamicSharedMemorySize, ATT_LDS);
  attn_kernel<<<256, 256, ATT_LDS, stream>>>(x, wq, kc, vc, wo, part);
  reduce_kernel<<<672, 256, 0, stream>>>(part, out);
}
```

```cpp
#include <hip/hip_runtime.h>
#include <hip/hip_bf16.h>

typedef unsigned short u16;
typedef __attribute__((ext_vector_type(8))) short bf16x8;
typedef __attribute__((ext_vector_type(4))) short bf16x4;
typedef __attribute__((ext_vector_type(4))) float f32x4;
typedef __attribute__((ext_vector_type(2))) float f32x2;
typedef __attribute__((ext_vector_type(2))) __bf16 bf16x2_t;
typedef __attribute__((ext_vector_type(4))) unsigned u32x4;
typedef __attribute__((ext_vector_type(2))) unsigned u32x2;
typedef __attribute__((address_space(3))) bf16x4* lds_b64_ptr;

#define DI __device__ __forceinline__

constexpr int NB = 8, SQ = 192, DM = 448, NH = 16, SK = 1024, DH = 256;
constexpr int HD = NH * DH;

DI unsigned cvtpk(float lo, float hi) {
  f32x2 v = {lo, hi};
  bf16x2_t b = __builtin_convertvector(v, bf16x2_t);
  return __builtin_bit_cast(unsigned, b);
}
DI bf16x4 tr_read(const char* p) {
  return __builtin_amdgcn_ds_read_tr16_b64_v4i16((lds_b64_ptr)(p));
}

constexpr int SLOT = 32768;
constexpr int NSLOT = 5;
constexpr int ATT_LDS = SLOT * NSLOT;
constexpr float SCL = 0.0625f * 1.4426950408889634f;
#define ATT_THR 40.0f
constexpr float THR = ATT_THR;

#define WAIT_VM(n) asm volatile("s_waitcnt vmcnt(" #n ") lgkmcnt(0)" ::: "memory")
#define WAIT_VMO(n) asm volatile("s_waitcnt vmcnt(" #n ")" ::: "memory")
#define BARRIER() __builtin_amdgcn_s_barrier()

#define SCHED() __builtin_amdgcn_sched_barrier(0)
#define STAMPL(k)
#define STAMP_END2()
#define STAMP(k)
#define STAMP_INIT()
#define STAMP_END()
DI void mfma_acc(f32x4& d, bf16x8 a, bf16x8 b) {
  asm volatile("v_mfma_f32_16x16x32_bf16 %0, %1, %2, %0" : "+a"(d) : "v"(a), "v"(b));
}
DI void mfma_v(f32x4& d, bf16x8 a, bf16x8 b) {
  asm volatile("v_mfma_f32_16x16x32_bf16 %0, %1, %2, %0" : "+v"(d) : "v"(a), "v"(b));
}
DI void mfma_v0(f32x4& d, bf16x8 a, bf16x8 b) {
  asm volatile("v_mfma_f32_16x16x32_bf16 %0, %1, %2, 0" : "=&v"(d) : "v"(a), "v"(b));
}

DI void mfma_acc0(f32x4& d, bf16x8 a, bf16x8 b) {
  asm volatile("v_mfma_f32_16x16x32_bf16 %0, %1, %2, 0" : "=a"(d) : "v"(a), "v"(b));
}
DI void mfma_vc(f32x4& d, bf16x8 a, bf16x8 b, f32x4 c) {
  asm volatile("v_mfma_f32_16x16x32_bf16 %0, %1, %2, %3" : "=&v"(d) : "v"(a), "v"(b), "v"(c));
}
#define PIN8A(x) "+a"((x)[0]), "+a"((x)[1]), "+a"((x)[2]), "+a"((x)[3]), "+a"((x)[4]), "+a"((x)[5]), "+a"((x)[6]), "+a"((x)[7])
#define PIN7A(x) "+a"((x)[0]), "+a"((x)[1]), "+a"((x)[2]), "+a"((x)[3]), "+a"((x)[4]), "+a"((x)[5]), "+a"((x)[6])
DI float max3f(float a, float b, float c) { float r; asm("v_max3_f32 %0, %1, %2, %3" : "=v"(r) : "v"(a), "v"(b), "v"(c)); return r; }
DI float fadd_s(float a, float b) { float r; asm("v_add_f32_e32 %0, %1, %2" : "=v"(r) : "v"(a), "v"(b)); return r; }

template <int OFF>
DI void dma_piece(unsigned ldsdst, unsigned voff, __amdgpu_buffer_rsrc_t rsrc, unsigned soff) {
  asm volatile("s_mov_b32 m0, %0\n\ts_nop 0\n\tbuffer_load_dwordx4 %1, %2, %3 offen offset:%4 nt lds"
               :: "s"(ldsdst), "v"(voff), "s"(rsrc), "s"(soff), "i"(OFF) : "memory");
}
DI void dma_piece_w(unsigned ldsdst, unsigned voff, __amdgpu_buffer_rsrc_t rsrc, unsigned soff) {
  asm volatile("s_mov_b32 m0, %0\n\ts_nop 0\n\tbuffer_load_dwordx4 %1, %2, %3 offen lds"
               :: "s"(ldsdst), "v"(voff), "s"(rsrc), "s"(soff) : "memory");
}
DI void dma_touch(unsigned ldsdst, unsigned voff, __amdgpu_buffer_rsrc_t rsrc) {
  asm volatile("s_mov_b32 m0, %0\n\ts_nop 0\n\tbuffer_load_dword %1, %2, 0 offen lds" :: "s"(ldsdst), "v"(voff), "s"(rsrc) : "memory");
}
DI float xmax_rows(float v) {
  auto a = __builtin_amdgcn_permlane16_swap(__float_as_uint(v), __float_as_uint(v), false, false);
  v = fmaxf(__uint_as_float(a[0]), __uint_as_float(a[1]));
  auto c = __builtin_amdgcn_permlane32_swap(__float_as_uint(v), __float_as_uint(v), false, false);
  return fmaxf(__uint_as_float(c[0]), __uint_as_float(c[1]));
}
DI float xsum_rows(float v) {
  auto a = __builtin_amdgcn_permlane16_swap(__float_as_uint(v), __float_as_uint(v), false, false);
  v = __uint_as_float(a[0]) + __uint_as_float(a[1]);
  auto c = __builtin_amdgcn_permlane32_swap(__float_as_uint(v), __float_as_uint(v), false, false);
  return __uint_as_float(c[0]) + __uint_as_float(c[1]);
}

__global__ __launch_bounds__(256, 1) void attn_kernel(const u16* __restrict__ X, const u16* __restrict__ Wq,
                                                      const u16* __restrict__ Kc, const u16* __restrict__ Vc,
                                                      const u16* __restrict__ Wo, u16* __restrict__ Part) {
  extern __shared__ __attribute__((aligned(16))) char smem[];
  STAMP_INIT();
  {
    unsigned long long pc; unsigned csz;
    asm volatile("s_getpc_b64 %0\n.Lattn_code_begin:\n\ts_mov_b32 %1, .Lall_code_end-.Lattn_code_begin" : "=s"(pc), "=s"(csz));
    __amdgpu_buffer_rsrc_t rC = __builtin_amdgcn_make_buffer_rsrc((void*)pc, (short)0, (int)(csz <= 65536u ? csz : 0u), 0x00020000);
    dma_touch((unsigned)(size_t)smem + 90112u, (unsigned)threadIdx.x * 128u + (blockIdx.x & 8u) * 4096u, rC);
  }
  const int tid = threadIdx.x, lane = tid & 63;
  const int wid = __builtin_amdgcn_readfirstlane(tid >> 6);
  const int rg = wid & 1, kh = wid >> 1;
  const int bid = blockIdx.x;
  const int idx = bid >> 3, rh = idx & 1, h = 2 * (bid & 7) + ((idx >> 1) & 1), b = idx >> 2;
  const int g = lane >> 4, i16 = lane & 15;
  const unsigned ldsb = (unsigned)(size_t)smem;
  const int q0 = rh * 96 + rg * 48;
  const unsigned hoff = __builtin_amdgcn_readfirstlane((unsigned)h * 512u);
  const char* Kbase = (const char*)Kc + (size_t)(b * NH + h) * SK * DH * 2;
  const char* Vbase = (const char*)Vc + (size_t)(b * NH + h) * SK * DH * 2;
  __amdgpu_buffer_rsrc_t rK = __builtin_amdgcn_make_buffer_rsrc((void*)Kbase, (short)0, SK * DH * 2, 0x00020000);
  __amdgpu_buffer_rsrc_t rV = __builtin_amdgcn_make_buffer_rsrc((void*)Vbase, (short)0, SK * DH * 2, 0x00020000);
  __amdgpu_buffer_rsrc_t rW = __builtin_amdgcn_make_buffer_rsrc((void*)Wq, (short)0, DM * HD * 2, 0x00020000);
  __amdgpu_buffer_rsrc_t rWo = __builtin_amdgcn_make_buffer_rsrc((void*)(Wo + (size_t)h * DH * DM), (short)0, DH * DM * 2, 0x00020000);
  const unsigned wsoff = (unsigned)wid * 8192u;
  unsigned kdo[8], vdo[4], wdo[4];
  {
    const unsigned hi = lane >> 5, l32 = lane & 31;
#pragma unroll
    for (int j = 0; j < 8; ++j) kdo[j] = hi * 512u + 16u * (l32 ^ (2u * j + hi));
#pragma unroll
    for (int j = 0; j < 4; ++j) vdo[j] = hi * 512u + 16u * (l32 ^ ((2u * j + hi) << 1));
#pragma unroll
    for (int j = 0; j < 4; ++j) wdo[j] = hi * 8192u + 16u * (l32 ^ (((2u * (j & 1) + hi) | ((j >> 1) << 2)) << 1));
  }
  auto issueK = [&](int tl, int slot, auto jt) {
    constexpr int j = decltype(jt)::value;
    dma_piece<(j & 3) * 1024>(ldsb + (unsigned)slot * SLOT + wsoff + 4096u * (j >> 2), kdo[j], rK,
                              (unsigned)tl * 32768u + wsoff + 4096u * (j >> 2));
  };
  auto issueV = [&](int tl, int slot, auto jt) {
    constexpr int j = decltype(jt)::value;
    dma_piece<(j & 3) * 1024>(ldsb + (unsigned)slot * SLOT + wsoff + 4096u * (j >> 2), vdo[j & 3], rV,
                              (unsigned)tl * 32768u + wsoff + 4096u * (j >> 2));
  };
  auto issueW = [&](int c, int slot, int j) {
    dma_piece_w(ldsb + (unsigned)slot * SLOT + wsoff + 1024u * j, wdo[(j & 1) + 2 * (j >> 2)], rW,
                (unsigned)(64 * c + 16 * wid + 2 * j) * 8192u + hoff);
  };
#define FOR8(J, ...) { { constexpr int J = 0; __VA_ARGS__ } { constexpr int J = 1; __VA_ARGS__ } { constexpr int J = 2; __VA_ARGS__ } { constexpr int J = 3; __VA_ARGS__ } \
                       { constexpr int J = 4; __VA_ARGS__ } { constexpr int J = 5; __VA_ARGS__ } { constexpr int J = 6; __VA_ARGS__ } { constexpr int J = 7; __VA_ARGS__ } }
#define IC(x) std::integral_constant<int, (x)>{}

  {
    __amdgpu_buffer_rsrc_t rX = __builtin_amdgcn_make_buffer_rsrc((void*)(X + (size_t)(b * SQ + rh * 96) * DM), (short)0, 96 * DM * 2, 0x00020000);
#pragma unroll
    for (int j = 0; j < 21; ++j) {
      const unsigned n = (unsigned)(wid * 21 + j) * 64u + lane, r = n / 56u, c = n - r * 56u;
      dma_piece_w(ldsb + (unsigned)(wid * 21 + j) * 1024u, r * 896u + 16u * (c ^ ((r >> 1) & 7u)), rX, 0u);
    }
  }
#pragma unroll
  for (int c = 0; c < 2; ++c)
#pragma unroll
    for (int j = 0; j < 8; ++j) issueW(c, c + 3, j);
  {
    const unsigned tdst = ldsb + 90112u + (unsigned)wid * 1024u;
    const unsigned line = (unsigned)((b * 2 + rh) * 112 + (tid & 127));
    const bool on = (tid & 127) < 112;
    if (wid < 2) dma_touch(tdst + 256u, on ? line * 128u : 0x40000000u, rWo);
    else dma_touch(tdst + 256u, on ? (line >> 2) * 8192u + (line & 3u) * 128u + hoff : 0x40000000u, rW);
  }

  unsigned koff[4], voff[8];
#pragma unroll
  for (int a = 0; a < 4; ++a) koff[a] = (unsigned)(kh * 32 + i16) * 512u + 16u * (unsigned)((4 * a + g) ^ i16);
  {
    const int row = 4 * g + (i16 >> 2), r7 = row & 7;
#pragma unroll
    for (int bb = 0; bb < 8; ++bb) voff[bb] = (unsigned)(kh * 32 + row) * 512u + (unsigned)kh * 256u + 32u * (unsigned)(bb ^ r7) + 8u * (unsigned)(i16 & 3);
  }

  const int xoff = 256 - 512 * kh;

  bf16x8 qf[3][8];
  {
    f32x4 qa[3][8];
    unsigned woff[8];
    {
      const int q4 = i16 >> 2, f = q4 | ((g & 1) << 2);
#pragma unroll
      for (int db = 0; db < 8; ++db) woff[db] = (unsigned)(8 * g + q4) * 512u + (unsigned)kh * 256u + 32u * (unsigned)(db ^ f) + 8u * (unsigned)(i16 & 3);
    }
    bf16x8 xf[3][14];
    STAMP(0);
    WAIT_VM(16);
    BARRIER();
    STAMP(1);
#pragma unroll
    for (int qb = 0; qb < 3; ++qb) {
      const unsigned r = (unsigned)(rg * 48 + qb * 16 + i16);
#pragma unroll
      for (int kk = 0; kk < 14; ++kk)
        xf[qb][kk] = *(const bf16x8*)(smem + r * 896u + 128u * (kk >> 1) + 16u * ((unsigned)(4 * (kk & 1) + g) ^ ((r >> 1) & 7u)));
    }
    bf16x4 wplo[4], wphi[4];
    auto qstep = [&](auto ctag) {
      constexpr int c = decltype(ctag)::value;
      STAMPL(2);
      if (c == 0) WAIT_VM(0); else WAIT_VMO(16);
      BARRIER();
      STAMPL(3);
      const char* wb0 = smem + ((c + 3) % 5) * SLOT;
      const char* wn0 = smem + ((c + 4) % 5) * SLOT;
      constexpr int PF = 4;
      bf16x4 wlo[16], whi[16];
      auto wread = [&](int i) {
        const char* p = wb0 + woff[i & 7] + (i >> 3) * 16384;
        wlo[i] = tr_read(p); whi[i] = tr_read(p + 2048);
      };
#pragma unroll
      for (int i = 0; i < PF; ++i) { if (c == 0) wread(i); else { wlo[i] = wplo[i]; whi[i] = wphi[i]; } }
      SCHED();
#pragma unroll
      for (int i = 0; i < 16; ++i) {
        bf16x8 wf = __builtin_shufflevector(wlo[i], whi[i], 0, 1, 2, 3, 4, 5, 6, 7);
#pragma unroll
        for (int qb = 0; qb < 3; ++qb) {
          if (qb == 0) {
            if (i + PF < 16) wread(i + PF);
            else if (c < 6) { wplo[i + PF - 16] = tr_read(wn0 + woff[i + PF - 16]); wphi[i + PF - 16] = tr_read(wn0 + woff[i + PF - 16] + 2048); }
          }
          if (c == 0) {
            if (qb == 1) { if (i < 8) issueW(2, 0, i); else issueW(4, 2, i - 8); }
            if (qb == 2 && i < 8) issueW(3, 1, i);
          } else if (qb == 2 && (i & 1)) {
            if (c < 3) issueW(c + 4, (c + 7) % 5, i >> 1);
            FOR8(J, if (J == (i >> 1)) { if (c == 3) issueK(0, 0, IC(J)); if (c == 4) issueK(1, 1, IC(J)); if (c == 5) issueV(0, 2, IC(J)); })
          }
          if (c == 0 && i < 8) mfma_acc0(qa[qb][i & 7], wf, xf[qb][0]);
          else mfma_acc(qa[qb][i & 7], wf, xf[qb][2 * c + (i >> 3)]);
          SCHED();
        }
      }
    };
    qstep(IC(0));
    qstep(IC(1)); qstep(IC(2)); qstep(IC(3)); qstep(IC(4)); qstep(IC(5)); qstep(IC(6));
    STAMP(2);
    asm volatile("s_nop 7\n\ts_nop 7" : PIN8A(qa[0]), PIN8A(qa[1]), PIN8A(qa[2]));
    asm volatile("s_waitcnt lgkmcnt(0)" ::: "memory");
    BARRIER();
#pragma unroll
    for (int qb = 0; qb < 3; ++qb)
#pragma unroll
      for (int db = 0; db < 8; ++db) {
        u32x2 w = {cvtpk(qa[qb][db][0] * SCL, qa[qb][db][1] * SCL), cvtpk(qa[qb][db][2] * SCL, qa[qb][db][3] * SCL)};
        const unsigned ch = 2u * (8u * kh + db) + (g >> 1);
        *(u32x2*)(smem + 3 * SLOT + (unsigned)(rg * 48 + qb * 16 + i16) * 512u + 16u * (ch ^ (unsigned)i16) + 8u * (g & 1)) = w;
      }
    asm volatile("s_waitcnt lgkmcnt(0)" ::: "memory");
    BARRIER();
#pragma unroll
    for (int qb = 0; qb < 3; ++qb)
#pragma unroll
      for (int ks = 0; ks < 8; ++ks)
        qf[qb][ks] = *(const bf16x8*)(smem + 3 * SLOT + (unsigned)(rg * 48 + qb * 16 + i16) * 512u + 16u * ((unsigned)(4 * ks + g) ^ (unsigned)i16));
  }

  f32x4 o[3][16];
  float mrun[3] = {0.f, 0.f, 0.f}, lsum[3], mtrue[3];
  f32x4 negm[3];
  int attempt = 0;
  int sl_c = 0, sl_i = 0;
  auto nxt = [](int x) { return x == NSLOT - 1 ? 0 : x + 1; };
  f32x4 sA[3][2], sB[3][2];
  bf16x8 pf[3];
#define ATT_PF 4
  constexpr int PF = ATT_PF;
  bf16x8 kpre[PF];
  bf16x4 vplo[PF], vphi[PF];

  constexpr unsigned WOS = 49152u;
  unsigned wodo[7];
  auto issueWo = [&](int ks, int j) {
    dma_piece_w(ldsb + WOS + (unsigned)((ks + 2) & 3) * 28672u + (unsigned)(wid * 7 + j) * 1024u, wodo[j], rWo, (unsigned)ks * 28672u);
  };

  auto phaseA = [&](f32x4 (&sn)[3][2], f32x4 (&sp)[3][2], int tk, auto mode_tag) {
    constexpr bool FIRST = decltype(mode_tag)::value == 1;
    constexpr bool ISSUE = decltype(mode_tag)::value == 0;
    if constexpr (FIRST) STAMP(4); else STAMPL(8);
    if constexpr (FIRST) WAIT_VM(8); else WAIT_VMO(16);
    BARRIER();
    STAMPL(5);
    const char* kb0 = smem + (unsigned)sl_c * SLOT;
    const char* nb0 = smem + (unsigned)nxt(sl_c) * SLOT;
    bf16x8 kf[16];
    unsigned pw[12];
    auto kread = [&](int i) { kf[i] = *(const bf16x8*)(kb0 + koff[(i >> 1) & 3] + (i >> 3) * 256 + (i & 1) * 8192); };
    if constexpr (FIRST) {
#pragma unroll
      for (int i = 0; i < PF; ++i) kread(i);
    } else {
#pragma unroll
      for (int i = 0; i < PF; ++i) kf[i] = kpre[i];
    }
    SCHED();
#pragma unroll
    for (int i = 0; i < 16; ++i) {
      const int ks = i >> 1, kb = i & 1;
#pragma unroll
      for (int qb = 0; qb < 3; ++qb) {
        const int m = 3 * i + qb, u = m % 6, blk = m / 6;
        if (qb == 0) {
          if (i + PF < 16) kread(i + PF);
          else if constexpr (FIRST) kpre[i + PF - 16] = *(const bf16x8*)(nb0 + koff[((i + PF - 16) >> 1) & 3] + ((i + PF - 16) & 1) * 8192);
          else { const char* p = nb0 + voff[i + PF - 16]; vplo[i + PF - 16] = tr_read(p); vphi[i + PF - 16] = tr_read(p + 8192); }
          if constexpr (!FIRST) {
            const int done = 3 * blk + (u == 3 ? 2 : 0), prev = (u == 3) ? 3 * blk : (blk > 0 ? 3 * (blk - 1) + 2 : 0);
#pragma unroll
            for (int k = 0; k < 12; ++k)
              if (2 * k + 2 <= done && 2 * k + 2 > prev) pw[k] = cvtpk(sp[k >> 2][(k >> 1) & 1][2 * (k & 1)], sp[k >> 2][(k >> 1) & 1][2 * (k & 1) + 1]);
          }
        }
        if (u == 5) {
          if constexpr (ISSUE) { FOR8(J, if (J == blk) issueK(tk, sl_i, IC(J));) }
        }
        if constexpr (FIRST) { if (qb == 2) { FOR8(J, if (J == i) issueK(2, 3, IC(J)); if (J + 8 == i) issueV(1, 4, IC(J));) } }
        if constexpr (!FIRST) {
          if (u == 1 || u == 2 || u == 4) {
            const int e = 3 * blk + (u == 1 ? 0 : u == 2 ? 1 : 2), eq = e >> 3, ej = e & 7;
            sp[eq][ej >> 2][ej & 3] = __builtin_amdgcn_exp2f(sp[eq][ej >> 2][ej & 3]);
          }
        }
        if (ks == 0) { if constexpr (FIRST) mfma_v0(sn[qb][kb], kf[i], qf[qb][ks]); else mfma_vc(sn[qb][kb], kf[i], qf[qb][ks], negm[qb]); }
        else mfma_v(sn[qb][kb], kf[i], qf[qb][ks]);
        SCHED();
      }
    }
    if constexpr (!FIRST) {
      pw[11] = cvtpk(sp[2][1][2], sp[2][1][3]);
#pragma unroll
      for (int qb = 0; qb < 3; ++qb) {
        u32x4 w = {pw[4 * qb], pw[4 * qb + 1], pw[4 * qb + 2], pw[4 * qb + 3]};
        pf[qb] = __builtin_bit_cast(bf16x8, w);
      }
      sl_i = nxt(sl_i);
    }
    sl_c = nxt(sl_c);
  };
  auto softmax_only = [&](f32x4 (&sp)[3][2]) {
#pragma unroll
    for (int qb = 0; qb < 3; ++qb) {
#pragma unroll
      for (int e = 0; e < 8; ++e) sp[qb][e >> 2][e & 3] = __builtin_amdgcn_exp2f(sp[qb][e >> 2][e & 3]);
      u32x4 w = {cvtpk(sp[qb][0][0], sp[qb][0][1]), cvtpk(sp[qb][0][2], sp[qb][0][3]), cvtpk(sp[qb][1][0], sp[qb][1][1]), cvtpk(sp[qb][1][2], sp[qb][1][3])};
      pf[qb] = __builtin_bit_cast(bf16x8, w);
    }
  };
  auto phaseB = [&](f32x4 (&sn)[3][2], f32x4 (&sp)[3][2], int tv, auto mode_tag) {
    constexpr int MODE = decltype(mode_tag)::value;
    STAMPL(6);
    if constexpr (MODE == 2) WAIT_VM(14); else WAIT_VMO(16);
    BARRIER();
    STAMPL(7);
    const char* vb0 = smem + (unsigned)sl_c * SLOT;
    const char* nb0 = smem + (unsigned)nxt(sl_c) * SLOT;
    bf16x4 vlo[16], vhi[16];
    auto vread = [&](int db) {
      const char* p = vb0 + voff[db & 7] + (db >> 3) * xoff;
      vlo[db] = tr_read(p); vhi[db] = tr_read(p + 8192);
    };
    if constexpr (MODE == 2) {
#pragma unroll
      for (int i = 0; i < PF; ++i) vread(i);
    } else {
#pragma unroll
      for (int i = 0; i < PF; ++i) { vlo[i] = vplo[i]; vhi[i] = vphi[i]; }
    }
    SCHED();
#pragma unroll
    for (int db = 0; db < 16; ++db) {
      bf16x8 vf = __builtin_shufflevector(vlo[db], vhi[db], 0, 1, 2, 3, 4, 5, 6, 7);
#pragma unroll
      for (int qb = 0; qb < 3; ++qb) {
        const int m = 3 * db + qb, u = m % 6, blk = m / 6;
        if (qb == 0) {
          if (db + PF < 16) vread(db + PF);
          else if constexpr (MODE == 0) kpre[db + PF - 16] = *(const bf16x8*)(nb0 + koff[((db + PF - 16) >> 1) & 3] + ((db + PF - 16) & 1) * 8192);
        }
        if constexpr (MODE == 0) { if (u == 5) { FOR8(J, if (J == blk) issueV(tv, sl_i, IC(J));) } }
        if constexpr (MODE == 1) { if (qb == 2 && db < 14) issueWo(db / 7, db % 7); }
        if (u == 1 || u == 2) {
#pragma unroll
          for (int z = 0; z < (u == 1 ? 2 : 1); ++z) {
            const int e = 3 * blk + (u == 1 ? z : 2), eq = e >> 3, ej = e & 7;
            lsum[eq] = fadd_s(lsum[eq], sp[eq][ej >> 2][ej & 3]);
          }
        }
        if constexpr (MODE < 2) {
          if (u == 2 || (u == 4 && (blk & 1) == 0)) {
            const int x = (3 * blk) / 2 + (u == 4 ? 1 : ((blk & 1) ? 1 : 0)) , eq = x >> 2, ej = x & 3;
            mtrue[eq] = max3f(mtrue[eq], sn[eq][ej >> 1][(ej & 1) * 2], sn[eq][ej >> 1][(ej & 1) * 2 + 1]);
          }
        }
        mfma_acc(o[qb][db], vf, pf[qb]);
        SCHED();
      }
    }
    sl_c = nxt(sl_c); sl_i = nxt(sl_i);
  };
  using T_ = std::true_type;
  using F_ = std::false_type;
  char* myreg = smem + wid * 26624;
  char* preg = smem + (wid ^ 2) * 26624;

  for (;; ++attempt) {
    sl_c = 0; sl_i = 0;
    {
      bf16x8 z = {0, 0, 0, 0, 0, 0, 0, 0};
      asm volatile("s_nop 1" : "+v"(z));
#pragma unroll
      for (int qb = 0; qb < 3; ++qb) {
        lsum[qb] = 0.f;
#pragma unroll
        for (int db = 0; db < 16; ++db) mfma_acc0(o[qb][db], z, z);
      }
    }
    phaseA(sA, sB, 0, IC(1));
    asm volatile("s_nop 7" ::: "memory");
    SCHED();
#pragma unroll
    for (int qb = 0; qb < 3; ++qb) {
      asm volatile("" : "+v"(sA[qb][0]), "+v"(sA[qb][1]));
      float r = max3f(max3f(sA[qb][0][0], sA[qb][0][1], sA[qb][0][2]), max3f(sA[qb][0][3], sA[qb][1][0], sA[qb][1][1]),
                      fmaxf(sA[qb][1][2], sA[qb][1][3]));
      if (attempt == 0) mrun[qb] = xmax_rows(r);
      mtrue[qb] = r - mrun[qb];
      negm[qb] = (f32x4){-mrun[qb], -mrun[qb], -mrun[qb], -mrun[qb]};
      sA[qb][0] += negm[qb]; sA[qb][1] += negm[qb];
    }
#pragma unroll 1
    for (int t = 0; t < 14; t += 2) {
      phaseA(sB, sA, t + 3, IC(0));
      phaseB(sB, sA, t + 2, IC(0));
      phaseA(sA, sB, t + 4, IC(0));
      phaseB(sA, sB, t + 3, IC(0));
    }
    phaseA(sB, sA, 17, IC(2));
    {
      unsigned lz = lane;
      asm volatile("" : "+v"(lz));
#pragma unroll
      for (int j = 0; j < 7; ++j) {
        const unsigned n = (unsigned)(wid * 7 + j) * 64u + lz, r = n / 56u, c = n - r * 56u;
        wodo[j] = r * 896u + 16u * (c ^ (((r >> 1) & 3u) << 1));
      }
    }
    phaseB(sB, sA, 16, IC(1));
    softmax_only(sB);
    sl_c = nxt(sl_c);
    phaseB(sA, sB, 17, IC(2));

    STAMP(8);
    bool bad = false;
#pragma unroll
    for (int qb = 0; qb < 3; ++qb) {
      lsum[qb] = xsum_rows(lsum[qb]);
      mtrue[qb] = xmax_rows(mtrue[qb]);
      bad |= !(mtrue[qb] <= THR);
    }
    const int wbad = (attempt == 0 && __any(bad)) ? 1 : 0;
    asm volatile("s_nop 7\n\ts_nop 7" : PIN8A(o[0]), PIN8A(o[1]), PIN8A(o[2]));
    asm volatile("" : PIN8A(o[0] + 8), PIN8A(o[1] + 8), PIN8A(o[2] + 8));
    asm volatile("s_waitcnt lgkmcnt(0)" ::: "memory");
    BARRIER();
#pragma unroll
    for (int qb = 0; qb < 3; ++qb)
#pragma unroll
      for (int d = 0; d < 8; ++d) *(f32x4*)(myreg + (qb * 8 + d) * 1024 + lane * 16) = o[qb][8 + d];
#pragma unroll
    for (int qb = 0; qb < 3; ++qb) *(f32x2*)(myreg + 24576 + qb * 512 + lane * 8) = (f32x2){mrun[qb], lsum[qb]};
    if (lane == 0) *(int*)(myreg + 26112) = wbad;
    asm volatile("s_waitcnt lgkmcnt(0)" ::: "memory");
    BARRIER();
    int f = *(const int*)(smem + 26112) | *(const int*)(smem + 26624 + 26112) | *(const int*)(smem + 2 * 26624 + 26112) |
            *(const int*)(smem + 3 * 26624 + 26112);
    f = __builtin_amdgcn_readfirstlane(f);
    if (!f) break;
    asm volatile("s_waitcnt vmcnt(0) lgkmcnt(0)" ::: "memory");
    BARRIER();
#pragma unroll
    for (int qb = 0; qb < 3; ++qb) mrun[qb] += mtrue[qb];
    FOR8(J, issueK(0, 0, IC(J));)
    FOR8(J, issueK(1, 1, IC(J));)
    FOR8(J, issueV(0, 2, IC(J));)
  }

  STAMP(9);
  u32x4 ob[3][4];
  float fa0[3], fa1[3];
#pragma unroll
  for (int qb = 0; qb < 3; ++qb) {
    f32x2 ml = *(const f32x2*)(preg + 24576 + qb * 512 + lane * 8);
    float mn = fmaxf(mrun[qb], ml[0]);
    float a0 = __builtin_amdgcn_exp2f(mrun[qb] - mn), a1 = __builtin_amdgcn_exp2f(ml[0] - mn);
    float inv = 1.0f / (lsum[qb] * a0 + ml[1] * a1);
    fa0[qb] = a0 * inv; fa1[qb] = a1 * inv;
  }
  auto combine = [&](auto off_tag) {
    constexpr int OFF = decltype(off_tag)::value;
#pragma unroll
    for (int qb = 0; qb < 3; ++qb)
#pragma unroll
      for (int d = 0; d < 8; ++d) {
        f32x4 pv = *(const f32x4*)(preg + (qb * 8 + d) * 1024 + lane * 16);
        f32x4 r = o[qb][OFF + d] * fa0[qb] + pv * fa1[qb];
        ob[qb][d >> 1][2 * (d & 1)] = cvtpk(r[0], r[1]); ob[qb][d >> 1][2 * (d & 1) + 1] = cvtpk(r[2], r[3]);
      }
  };
  combine(IC(0));
  asm volatile("s_waitcnt lgkmcnt(0)" ::: "memory");
  BARRIER();
#pragma unroll
  for (int qb = 0; qb < 3; ++qb)
#pragma unroll
    for (int dp = 0; dp < 4; ++dp)
      *(u32x4*)(smem + (unsigned)(rg * 48 + qb * 16 + i16) * 512u + 16u * ((unsigned)(kh * 16 + dp * 4 + g) ^ (unsigned)i16)) = ob[qb][dp];
#pragma unroll
  for (int j = 0; j < 7; ++j) issueWo(2, j);
#pragma unroll
  for (int j = 0; j < 7; ++j) issueWo(3, j);
  STAMP(10);

  {
    f32x4 pa[6][7];
    {
      bf16x8 z = {0, 0, 0, 0, 0, 0, 0, 0};
      asm volatile("s_nop 1" : "+v"(z));
#pragma unroll
      for (int mb = 0; mb < 6; ++mb)
#pragma unroll
        for (int nb = 0; nb < 7; ++nb) mfma_acc0(pa[mb][nb], z, z);
    }
    unsigned wof[7];
    {
      const unsigned r = (unsigned)(4 * g + (i16 >> 2));
#pragma unroll
      for (int nb = 0; nb < 7; ++nb) {
        const unsigned seg = (unsigned)(wid * 7 + nb);
        wof[nb] = WOS + r * 896u + 32u * (seg ^ ((r >> 1) & 3u)) + 8u * (unsigned)(i16 & 3);
      }
    }
    bf16x8 of[6], ofn[6];
    bf16x4 wlo[7], whi[7], wplo[3], wphi[3];
    STAMP(11);
    WAIT_VM(14);
    BARRIER();
    STAMP(12);
    {
      const char* ab = smem + (unsigned)i16 * 512u + 16u * ((unsigned)g ^ (unsigned)i16);
#pragma unroll
      for (int mb = 0; mb < 6; ++mb) ofn[mb] = *(const bf16x8*)(ab + mb * 8192);
#pragma unroll
      for (int nb = 0; nb < 3; ++nb) { wplo[nb] = tr_read(smem + 2 * 28672 + wof[nb]); wphi[nb] = tr_read(smem + 2 * 28672 + wof[nb] + 16 * 896); }
    }
#pragma unroll 1
    for (int ks = 0; ks < 8; ++ks) {
      STAMPL(13);
      if (ks > 0) { WAIT_VMO(7); BARRIER(); }
      STAMPL(12);
      const char* wb = smem + (unsigned)((ks + 2) & 3) * 28672u;
      const char* wn = smem + (unsigned)((ks + 3) & 3) * 28672u;
      const char* an = smem + (unsigned)i16 * 512u + 16u * ((unsigned)(4 * ((ks + 1) & 7) + g) ^ (unsigned)i16);
#pragma unroll
      for (int mb = 0; mb < 6; ++mb) of[mb] = ofn[mb];
#pragma unroll
      for (int nb = 0; nb < 3; ++nb) { wlo[nb] = wplo[nb]; whi[nb] = wphi[nb]; }
      SCHED();
#pragma unroll
      for (int nb = 0; nb < 7; ++nb) {
        bf16x8 wf = __builtin_shufflevector(wlo[nb], whi[nb], 0, 1, 2, 3, 4, 5, 6, 7);
#pragma unroll
        for (int mb = 0; mb < 6; ++mb) {
          if (mb == 1) {
            if (nb + 3 < 7) { wlo[nb + 3] = tr_read(wb + wof[nb + 3]); whi[nb + 3] = tr_read(wb + wof[nb + 3] + 16 * 896); }
            else { wplo[nb - 4] = tr_read(wn + wof[nb - 4]); wphi[nb - 4] = tr_read(wn + wof[nb - 4] + 16 * 896); }
          }
          if (mb == 3 && ks > 0) issueWo(ks + 3, nb);
          if (mb == 5 && nb < 6) ofn[nb] = *(const bf16x8*)(an + nb * 8192);
          mfma_acc(pa[mb][nb], wf, of[mb]);
          SCHED();
        }
      }
    }
    STAMP(13);
    asm volatile("s_nop 7\n\ts_nop 7" : PIN7A(pa[0]), PIN7A(pa[1]), PIN7A(pa[2]));
    asm volatile("" : PIN7A(pa[3]), PIN7A(pa[4]), PIN7A(pa[5]));
    u16* pp = Part + ((size_t)(h * 16 + b * 2 + rh) * 4 + wid) * (21 * 512) + lane * 8;
#pragma unroll
    for (int mp = 0; mp < 3; ++mp)
#pragma unroll
      for (int nb = 0; nb < 7; ++nb) {
        const f32x4 x0 = pa[2 * mp][nb], x1 = pa[2 * mp + 1][nb];
        u32x4 w = {cvtpk(x0[0], x0[1]), cvtpk(x0[2], x0[3]), cvtpk(x1[0], x1[1]), cvtpk(x1[2], x1[3])};
        __builtin_nontemporal_store(w, (u32x4*)(pp + (mp * 7 + nb) * 512));
      }
  }
  STAMP(14);
  STAMP_END();
}

__global__ __launch_bounds__(256) void reduce_kernel(const u16* __restrict__ Part, u16* __restrict__ out) {
  __shared__ __attribute__((aligned(16))) float red[2][64][8];
  const int wv = threadIdx.x >> 6, lane = threadIdx.x & 63, hh = wv >> 1;
  const int gw = blockIdx.x * 2 + (wv & 1);
  const u16* p = Part + (size_t)gw * 512 + lane * 8 + (size_t)hh * 8 * (16 * 4 * 21 * 512);
  u32x4 v[8];
#pragma unroll
  for (int h = 0; h < 8; ++h) v[h] = __builtin_nontemporal_load((const u32x4*)(p + (size_t)h * (16 * 4 * 21 * 512)));
  float a[8] = {0.f, 0.f, 0.f, 0.f, 0.f, 0.f, 0.f, 0.f};
#pragma unroll
  for (int h = 0; h < 8; ++h)
#pragma unroll
    for (int e = 0; e < 4; ++e) {
      a[2 * e] += __uint_as_float(v[h][e] << 16);
      a[2 * e + 1] += __uint_as_float(v[h][e] & 0xffff0000u);
    }
  if (hh) {
    *(f32x4*)&red[wv & 1][lane][0] = (f32x4){a[0], a[1], a[2], a[3]};
    *(f32x4*)&red[wv & 1][lane][4] = (f32x4){a[4], a[5], a[6], a[7]};
  }
  __syncthreads();
  if (!hh) {
    const f32x4 r0 = *(const f32x4*)&red[wv & 1][lane][0], r1 = *(const f32x4*)&red[wv & 1][lane][4];
    const int blk = gw % 21, rw = gw / 21, w = rw & 3, rt = rw >> 2, mp = blk / 7, nb = blk - mp * 7;
    u16* op = out + (size_t)(rt * 96 + mp * 32 + (lane & 15)) * DM + w * 112 + nb * 16 + (lane >> 4) * 4;
    *(u32x2*)op = (u32x2){cvtpk(a[0] + r0[0], a[1] + r0[1]), cvtpk(a[2] + r0[2], a[3] + r0[3])};
    *(u32x2*)(op + 16 * DM) = (u32x2){cvtpk(a[4] + r1[0], a[5] + r1[1]), cvtpk(a[6] + r1[2], a[7] + r1[3])};
  }
  asm volatile(".Lall_code_end:");
}

extern "C" void kernel_launch(void* const* d_in, const int* in_sizes, int n_in, void* d_out, int out_size,
                              void* d_ws, size_t ws_size, hipStream_t stream) {
  const u16* x = (const u16*)d_in[0];
  const u16* kc = (const u16*)d_in[1];
  const u16* vc = (const u16*)d_in[2];
  const u16* wq = (const u16*)d_in[3];
  const u16* wo = (const u16*)d_in[4];
  u16* out = (u16*)d_out;
  u16* part = (u16*)d_ws;
  (void)hipFuncSetAttribute((const void*)attn_kernel, hipFuncAttributeMaxDynamicSharedMemorySize, ATT_LDS);
  attn_kernel<<<256, 256, ATT_LDS, stream>>>(x, wq, kc, vc, wo, part);
  reduce_kernel<<<672, 256, 0, stream>>>(part, out);
}
```
